```python
import torch

def _softmax(x, axis=-1):
    return torch.softmax(x, dim=axis)
def _gelu(x):
    return torch.nn.functional.gelu(x, approximate="tanh")
def _silu(x):
    return torch.nn.functional.silu(x)
def _sigmoid(x):
    return torch.sigmoid(x)
def _layernorm(x, axis=-1, eps=1e-5):
    m = x.mean(dim=axis, keepdim=True)
    v = x.var(dim=axis, keepdim=True, unbiased=False)
    return (x - m) / torch.sqrt(v + eps)
def _scatter_add(tgt, idx, src, axis=0):
    return tgt.clone().index_add_(axis, idx, src)
def _segment_sum(src, seg, n):
    out = torch.zeros((n,) + src.shape[1:], dtype=src.dtype)
    return out.index_add_(0, seg, src)
def _pad(x, pw):
    flat = tuple(v for pair in reversed(pw) for v in pair)
    return torch.nn.functional.pad(x, flat)

kernel_name = 'parallel_sum__linear_kernel_hedgehog__dense_activation_squared_relu__prompt'

def setup_inputs():

    _w = (lambda a: (lambda t: t.to(torch.bfloat16) if t.is_floating_point() else t)(torch.as_tensor(a)))
    def _A_setup():
        B, S, D, H = 1, 2048, 640, 32
        Dh = D // H
        return {
            "x": _w((lambda *s: torch.randn(*s, dtype=torch.bfloat16) * 0.05)(B, S, D)),
            "wqkv": _w((lambda *s: torch.randn(*s, dtype=torch.bfloat16) * 0.8)(D, 3 * D)),
            "w_phi": _w((lambda *s: torch.randn(*s, dtype=torch.bfloat16) * 0.8)(H, Dh, Dh) * (Dh ** -0.5)),
            "wo": _w((lambda *s: torch.randn(*s, dtype=torch.bfloat16) * 0.2)(D, D)),
        }
    _A_in = _A_setup()

    def _B_setup():
        B, S, D, F = 1, 2048, 640, 1728
        return {
            "x": _w((lambda *s: torch.randn(*s, dtype=torch.bfloat16) * 0.05)(B, S, D)),
            "w1": _w((lambda *s: torch.randn(*s, dtype=torch.bfloat16) * 0.05)(D, F)),
            "b1": _w((lambda *s: torch.randn(*s, dtype=torch.bfloat16) * 0.8)(F)),
            "w2": _w((lambda *s: torch.randn(*s, dtype=torch.bfloat16) * 0.05)(F, D)),
            "b2": _w((lambda *s: torch.randn(*s, dtype=torch.bfloat16) * 3.2)(D)),
        }
    _B_in = _B_setup()

    return {
        "A_x": _A_in["x"],
        "A_wqkv": _A_in["wqkv"],
        "A_w_phi": _A_in["w_phi"],
        "A_wo": _A_in["wo"],
        "B_x": _B_in["x"],
        "B_w1": _B_in["w1"],
        "B_b1": _B_in["b1"],
        "B_w2": _B_in["w2"],
        "B_b2": _B_in["b2"],
    }

def reference(A_x, A_wqkv, A_w_phi, A_wo, B_x, B_w1, B_b1, B_w2, B_b2):
    def _A_ref(x, wqkv, w_phi, wo):
        B, S, D = x.shape
        H = 32; Dh = D // H
        qkv = (lambda x, *d: x.permute(*d))((x @ wqkv).reshape(B, S, 3, H, Dh), 2, 0, 3, 1, 4)
        q, k, v = qkv[0], qkv[1], qkv[2]
        pq = torch.einsum("bhsd,hdf->bhsf", q, w_phi)
        pk = torch.einsum("bhsd,hdf->bhsf", k, w_phi)
        phi_q = torch.concatenate([_softmax(pq, axis=-1), _softmax(-pq, axis=-1)], axis=-1)
        phi_k = torch.concatenate([_softmax(pk, axis=-1), _softmax(-pk, axis=-1)], axis=-1)
        kv = torch.einsum("bhsf,bhsd->bhfd", phi_k, v)
        z  = torch.einsum("bhsf->bhf", phi_k)
        num = torch.einsum("bhqf,bhfd->bhqd", phi_q, kv)
        den = torch.einsum("bhqf,bhf->bhq", phi_q, z)[..., None] + 1e-6
        o = num / den
        return (lambda x, *d: x.permute(*d))(o, 0, 2, 1, 3).reshape(B, S, D) @ wo

    def _B_ref(x, w1, b1, w2, b2):
        h = torch.einsum("bsd,df->bsf", x, w1) + b1
        zero = torch.zeros_like(h)
        r = torch.where(h > zero, h, zero)
        h = r * r
        out = torch.einsum("bsf,fd->bsd", h, w2) + b2
        return out

    x = A_x
    xn = _layernorm(x, axis=-1)
    return x + _A_ref(xn, A_wqkv, A_w_phi, A_wo) + _B_ref(xn, B_w1, B_b1, B_w2, B_b2)
```

```cpp
#include <hip/hip_runtime.h>
#include <hip/hip_bf16.h>
typedef unsigned short u16;
#define MIMIC 1
__device__ __forceinline__ float bf2f(u16 x){ return __uint_as_float(((unsigned)x)<<16); }
__device__ __forceinline__ u16 f2bf(float f){ unsigned u=__float_as_uint(f); u += 0x7fffu + ((u>>16)&1u); return (u16)(u>>16); }
__device__ __forceinline__ float rb(float f){
  return bf2f(f2bf(f));
}
constexpr int S=2048, D=640, H=32, DH=20, F=1728;

__global__ void ln_k(const u16* x, float* xn){
  int row=blockIdx.x; int t=threadIdx.x;
  float s=0; for(int i=t;i<D;i+=64) s+=bf2f(x[row*D+i]);
  for(int m=32;m>=1;m>>=1) s+=__shfl_xor(s,m);
  float mean=s/D;
  float v=0; for(int i=t;i<D;i+=64){ float d=bf2f(x[row*D+i])-mean; v+=d*d; }
  for(int m=32;m>=1;m>>=1) v+=__shfl_xor(v,m);
  v/=D;
  float mb=rb(mean), vb=rb(v);
  float sd=rb(sqrtf(rb(vb+1e-5f)));
  for(int i=t;i<D;i+=64){ float d=rb(bf2f(x[row*D+i])-mb); xn[row*D+i]=rb(d/sd); }
}
__global__ void gemm_k(const float* A, const u16* B, float* C, int M,int N,int K){
  __shared__ float sA[16][17], sB[16][17];
  int tx=threadIdx.x, ty=threadIdx.y; int n=blockIdx.x*16+tx, m=blockIdx.y*16+ty;
  float acc=0;
  for(int k0=0;k0<K;k0+=16){
    sA[ty][tx]=A[(size_t)m*K+k0+tx];
    sB[ty][tx]=bf2f(B[(size_t)(k0+ty)*N+n]);
    __syncthreads();
    for(int k=0;k<16;++k) acc+=sA[ty][k]*sB[k][tx];
    __syncthreads();
  }
  C[(size_t)m*N+n]=acc;
}
__global__ __launch_bounds__(256) void phi_k(const float* qkv, const u16* wphi, float* phi  ){
  int idx=blockIdx.x*blockDim.x+threadIdx.x; if(idx>=2*S*H) return;
  int which=idx/(S*H); int r=idx%(S*H); int s=r/H, h=r%H;
  float q[DH];
  #pragma unroll
  for(int d=0;d<DH;++d) q[d]=bf2f(f2bf(qkv[(size_t)s*3*D+which*D+h*DH+d]));
  float p[DH]; float mx=-1e30f, mn=1e30f;
  #pragma unroll
  for(int f=0;f<DH;++f){ float a=0;
    #pragma unroll
    for(int d=0;d<DH;++d) a+=q[d]*bf2f(wphi[h*DH*DH+d*DH+f]);
    a=rb(a); p[f]=a; mx=fmaxf(mx,a); mn=fminf(mn,a);}
  float sp=0,sn=0;
  #pragma unroll
  for(int f=0;f<DH;++f){ sp+=__expf(p[f]-mx); sn+=__expf(mn-p[f]); }
  float* o=phi+((size_t)which*S*H+r)*40;
  #pragma unroll
  for(int f=0;f<DH;++f){ o[f]=bf2f(f2bf(__expf(p[f]-mx)/sp)); o[20+f]=bf2f(f2bf(__expf(mn-p[f])/sn)); }
}
__global__ void kv_k(const float* qkv, const float* phi, float* kv){
  int h=blockIdx.x; int t=threadIdx.x; if(t>=840) return; int f=t/21, d=t%21;
  const float* pk=phi+(size_t)S*H*40;
  float a=0;
  for(int s=0;s<S;++s){ float v = d<20? bf2f(f2bf(qkv[(size_t)s*3*D+2*D+h*DH+d])) : 1.f; a+=pk[((size_t)s*H+h)*40+f]*v; }
  kv[h*840+t]=bf2f(f2bf(a));
}
__global__ void o_k(const float* phi, const float* kv, float* o){
  int idx=blockIdx.x*blockDim.x+threadIdx.x; if(idx>=S*D) return;
  int s=idx/D, c=idx%D, h=c/DH, d=c%DH;
  float num=0, den=0;
  for(int f=0;f<40;++f){ float p=phi[((size_t)s*H+h)*40+f]; num+=p*kv[h*840+f*21+d]; den+=p*kv[h*840+f*21+20]; }
  num=rb(num); den=rb(rb(den)+1e-6f);
  o[idx]=bf2f(f2bf(num/den));
}
__global__ void act_k(float* h, const u16* b1){
  int idx=blockIdx.x*blockDim.x+threadIdx.x; if(idx>=S*F) return;
  float v=rb(rb(h[idx])+bf2f(b1[idx%F])); v=v>0?v:0; h[idx]=bf2f(f2bf(v*v));
}
__global__ void fin_k(const u16* x, const float* a, const float* b, const u16* b2, u16* out){
  int idx=blockIdx.x*blockDim.x+threadIdx.x; if(idx>=S*D) return;
  float bo=rb(rb(b[idx])+bf2f(b2[idx%D]));
  float r=rb(bf2f(x[idx])+rb(a[idx]));
  out[idx]=f2bf(r+bo);
}
extern "C" void kernel_launch(void* const* d_in, const int* in_sizes, int n_in, void* d_out, int out_size, void* d_ws, size_t ws_size, hipStream_t stream){
  const u16* x=(const u16*)d_in[0]; const u16* wqkv=(const u16*)d_in[1]; const u16* wphi=(const u16*)d_in[2]; const u16* wo=(const u16*)d_in[3];
  const u16* w1=(const u16*)d_in[5]; const u16* b1=(const u16*)d_in[6]; const u16* w2=(const u16*)d_in[7]; const u16* b2=(const u16*)d_in[8];
  float* ws=(float*)d_ws;
  float* xn=ws; ws+=S*D; float* qkv=ws; ws+=S*3*D; float* phi=ws; ws+=2*S*H*40; float* kv=ws; ws+=H*840+64; float* o=ws; ws+=S*D; float* hh=ws; ws+=S*F; float* ao=ws; ws+=S*D; float* bo=ws; ws+=S*D;
  ln_k<<<S,64,0,stream>>>(x,xn);
  gemm_k<<<dim3(3*D/16,S/16),dim3(16,16),0,stream>>>(xn,wqkv,qkv,S,3*D,D);
  phi_k<<<(2*S*H+255)/256,256,0,stream>>>(qkv,wphi,phi);
  kv_k<<<H,1024,0,stream>>>(qkv,phi,kv);
  o_k<<<(S*D+255)/256,256,0,stream>>>(phi,kv,o);
  gemm_k<<<dim3(D/16,S/16),dim3(16,16),0,stream>>>(o,wo,ao,S,D,D);
  gemm_k<<<dim3(F/16,S/16),dim3(16,16),0,stream>>>(xn,w1,hh,S,F,D);
  act_k<<<(S*F+255)/256,256,0,stream>>>(hh,b1);
  gemm_k<<<dim3(D/16,S/16),dim3(16,16),0,stream>>>(hh,w2,bo,S,D,F);
  fin_k<<<(S*D+255)/256,256,0,stream>>>(x,ao,bo,b2,(u16*)d_out);
}
```

```cpp
#define CB 36256
#include <hip/hip_runtime.h>
#include <hip/hip_bf16.h>

typedef unsigned short u16;
typedef unsigned u32;
typedef unsigned long long u64;
typedef short s16x4 __attribute__((ext_vector_type(4)));
typedef __bf16 bf16x2 __attribute__((ext_vector_type(2)));
typedef __bf16 bf16x8 __attribute__((ext_vector_type(8)));
typedef float f32x2 __attribute__((ext_vector_type(2)));
typedef float f32x4 __attribute__((ext_vector_type(4)));
typedef float f32x8 __attribute__((ext_vector_type(8)));
typedef float f32x16 __attribute__((ext_vector_type(16)));
typedef float f32x32 __attribute__((ext_vector_type(32)));
typedef u32 u32x2 __attribute__((ext_vector_type(2)));
typedef u32 u32x4 __attribute__((ext_vector_type(4)));
#define LAS __attribute__((address_space(3)))
#define GAS __attribute__((address_space(1)))
#define DI __device__ __forceinline__
typedef __amdgpu_buffer_rsrc_t rsrc_t;

constexpr int S_ = 2048, D_ = 640, F_ = 1728;
constexpr int NKS2 = 74;
constexpr u32 WS_EPOCH = 0, WS_CSUM = 0x10000, CS_PLANE = 0x10000, WS_STATS = 0xFC0000, ST_PLANE = 0x10000;
#define OAUX 0
#define PD1 4
#define PD2 2
#define PDEP 1
#define PSL 6
#define CPF 1
#define SPF 1
#define R1F 3
#define MPR 3
#define KVO 5
#define PD3 1
#define QT 0
#define QN0 3
#define KW 1
#define WOT 0
#define QD 3
#define QE 2
#define QP 7
#define QM 7
#define FV 0
#define KT 1
#define KD 0
#define QL 0
#define HT 3
#define FW 0
#define HF 1
#define AT 0
#define OR 0
#define XT 1
#define L8 1
#define WK 0
#define HN 0
#define HPR 2
#define TILE 1
#define TPF 1
#define NE 1
#define WSTOP 0
constexpr unsigned CODE_BYTES = CB;

constexpr u32 WS_HF = 0x180000, WS_RED = 0x170000, WS_WO = 0x200000, WS_OF = 0x480000;
constexpr u32 WS_PART = 0x90000, PART_STRIDE = 1792;
constexpr u32 WS_XCH = 0x1000000;
constexpr u32 WS_ACT = 0x680000, ACT_PANEL = NKS2 * 4096;
constexpr u32 WS_END = 0x1800000;
constexpr u32 WS_STAMP = 0x2000000;
constexpr u32 SLOT = 47104, W_OFF = 16384, C_OFF = 36864;
constexpr u32 SL0 = 2 * SLOT, SL1 = 0, SL2 = SLOT;
constexpr u32 L_KI = 81920, L_QI = 122880;
constexpr u32 L_KVT = 143360, L_ZERO = 150080, L_ST = 151808, L_CS = 152832, L_BB = 153856, L_B2 = 154880;
constexpr u32 L_RAW = 157696, L_CTR = 162816;
constexpr u32 C_KV = L_CTR, C_RD = L_CTR + 4, C_F = L_CTR + 8, C_H = L_CTR + 12, C_X = L_CTR + 48, C_V = L_CTR + 52, C_W = L_CTR + 16, C_P = L_CTR + 32;
constexpr int LDS_BYTES = 163840;
constexpr u32 OOB = 0x40000000u;
constexpr u32 kSpinCap = 1u << 19;

DI f32x4 mfma32(bf16x8 a, bf16x8 b, f32x4 c) { return __builtin_amdgcn_mfma_f32_16x16x32_bf16(a, b, c, 0, 0, 0); }
DI u32 pk2(float a, float b) { f32x2 t = {a, b}; return __builtin_bit_cast(u32, __builtin_convertvector(t, bf16x2)); }
DI float rbf(float a) { return __uint_as_float(pk2(a, a) & 0xffff0000u); }
DI float blo(u32 w) { return __uint_as_float(w << 16); }
DI float bhi(u32 w) { return __uint_as_float(w & 0xffff0000u); }
DI bf16x8 mk8(u32 a, u32 b, u32 c, u32 d) { u32x4 t = {a, b, c, d}; return __builtin_bit_cast(bf16x8, t); }
DI bf16x8 as8(u32x4 t) { return __builtin_bit_cast(bf16x8, t); }
DI float dot2(u32 a, u32 b, float c) { return __builtin_amdgcn_fdot2_f32_bf16(__builtin_bit_cast(bf16x2, a), __builtin_bit_cast(bf16x2, b), c, false); }
DI u32x2 tr8(u32 addr) { return __builtin_bit_cast(u32x2, __builtin_amdgcn_ds_read_tr16_b64_v4i16((LAS s16x4*)addr)); }
DI bf16x8 tr16(u32 a0, u32 a1) { u32x2 x = tr8(a0), y = tr8(a1); return mk8(x.x, x.y, y.x, y.y); }
DI u32x4 lds128(u32 a) { return *(LAS u32x4*)a; }
DI f32x4 ldsf4(u32 a) { return *(LAS f32x4*)a; }
DI u32x2 lds64(u32 a) { return *(LAS u32x2*)a; }
DI u32 lds32(u32 a) { return *(LAS u32*)a; }
DI void sts128(u32 a, u32x4 v) { *(LAS u32x4*)a = v; }
DI void sts64(u32 a, u32x2 v) { *(LAS u32x2*)a = v; }
DI void sts32(u32 a, u32 v) { *(LAS u32*)a = v; }
DI u32 rfl(u32 v) { return __builtin_amdgcn_readfirstlane(v); }
DI rsrc_t mkrsrc(const void* p, u32 bytes) { return __builtin_amdgcn_make_buffer_rsrc((void*)p, 0, bytes, 0x00020000); }
DI void st128(u32x4 v, rsrc_t r, u32 off) { __builtin_amdgcn_raw_buffer_store_b128(v, r, off, 0, 16); }
DI void st64(u32x2 v, rsrc_t r, u32 off) { __builtin_amdgcn_raw_buffer_store_b64(v, r, off, 0, 16); }
DI void st32(u32 v, rsrc_t r, u32 off) { __builtin_amdgcn_raw_buffer_store_b32(v, r, off, 0, 16); }
DI void dma16(rsrc_t r, u32 voff, u32 soff, u32 ldsdst) {
  asm volatile("s_mov_b32 m0, %0\n\ts_nop 0\n\tbuffer_load_dwordx4 %1, %2, %3 offen lds" :: "s"(ldsdst), "v"(voff), "s"(r), "s"(soff) : "memory");
}
DI void dma16c(rsrc_t r, u32 voff, u32 soff, u32 ldsdst) {
  asm volatile("s_mov_b32 m0, %0\n\ts_nop 0\n\tbuffer_load_dwordx4 %1, %2, %3 offen sc1 lds" :: "s"(ldsdst), "v"(voff), "s"(r), "s"(soff) : "memory");
}
DI u32x4 ld128c(rsrc_t r, u32 voff, u32 soff) { u32x4 v;
  asm volatile("buffer_load_dwordx4 %0, %1, %2, %3 offen sc1" : "=v"(v) : "v"(voff), "s"(r), "s"(soff) : "memory"); return v; }
DI u32x4 ld128h(rsrc_t r, u32 voff, u32 soff) { u32x4 v;
  asm volatile("buffer_load_dwordx4 %0, %1, %2, %3 offen" : "=v"(v) : "v"(voff), "s"(r), "s"(soff) : "memory"); return v; }
DI void st128h(u32x4 v, rsrc_t r, u32 off) { asm volatile("buffer_store_dwordx4 %0, %1, %2, 0 offen sc1\n\ts_nop 1" :: "v"(v), "v"(off), "s"(r) : "memory"); }
DI u32 umax3(u32 a, u32 b, u32 c) { return __builtin_elementwise_max(__builtin_elementwise_max(a, b), c); }
DI u32x2 ld64c(rsrc_t r, u32 voff) { u32x2 v;
  asm volatile("buffer_load_dwordx2 %0, %1, %2, 0 offen sc1" : "=v"(v) : "v"(voff), "s"(r) : "memory"); return v; }
DI u32 ld16h(rsrc_t r, u32 voff) { u32 v;
  asm volatile("buffer_load_ushort %0, %1, %2, 0 offen" : "=v"(v) : "v"(voff), "s"(r) : "memory"); return v; }
template <class T> DI void pin(T& v) { asm volatile("" : "+v"(v)); }
#define wait_bar(N, k) asm volatile("s_waitcnt vmcnt(%0) lgkmcnt(0)\n\ts_barrier" :: "n"(N) : "memory")
DI void lbar() { asm volatile("s_waitcnt lgkmcnt(0)\n\ts_barrier" ::: "memory"); }
template <int N> DI void wait_vm() { asm volatile("s_waitcnt vmcnt(%0)" :: "n"(N) : "memory"); }
DI void lds_arrive(u32 ctr) {
  asm volatile("s_waitcnt lgkmcnt(0)\n\ts_mov_b64 exec, 1\n\tds_add_u32 %0, %1\n\ts_mov_b64 exec, -1" :: "v"(ctr), "v"(1u) : "memory");
}
DI void lds_wait(u32 ctr, u32 n) {
#pragma unroll 1
  for (u32 spins = 0; spins < kSpinCap; ++spins) {
    if (rfl(__hip_atomic_load((LAS u32*)ctr, __ATOMIC_RELAXED, __HIP_MEMORY_SCOPE_WORKGROUP)) >= n) break;
    __builtin_amdgcn_s_sleep(1); }
  asm volatile("" ::: "memory");
}
DI void put_flag(rsrc_t r, u32 off, u64 tag) { asm volatile("buffer_store_dwordx2 %0, %1, %2, 0 offen sc1" :: "v"(tag), "v"(off), "s"(r) : "memory"); }
template <int N> DI void publish(u32 ctr, rsrc_t r, u32 off, u64 tag) { u32 t;
  asm volatile("s_mov_b64 exec, 1\n\ts_waitcnt vmcnt(%6)\n\tds_add_rtn_u32 %0, %4, %5\n\ts_waitcnt lgkmcnt(0)\n\tv_cmp_eq_u32 vcc, 3, %0\n\ts_mov_b64 exec, -1\n\t"
               "s_cbranch_vccz 1f\n\tbuffer_store_dwordx2 %1, %2, %3, 0 offen sc1\n1:"
               : "=&v"(t) : "v"(tag), "v"(off), "s"(r), "v"(ctr), "v"(1u), "n"(N) : "memory", "vcc"); }
DI void sts_now(u32 a, u32 v) { asm volatile("ds_write_b32 %0, %1" :: "v"(a), "v"(v) : "memory"); }
#define PISS(x) "buffer_load_dwordx4 v[" #x ":" #x "+3], %[o0], %[r], 0 offen sc1\n\tbuffer_load_dwordx4 v[" #x "+4:" #x "+7], %[o1], %[r], 0 offen sc1\n\t"
#define PCHK(x, l) "s_waitcnt vmcnt(%[w])\n\tv_max3_u32 %[t], v[" #x "], v[" #x "+1], v[" #x "+2]\n\tv_max3_u32 %[u], v[" #x "+3], v[" #x "+4], v[" #x "+5]\n\t" \
  "v_max3_u32 %[t], %[t], v[" #x "+6], v[" #x "+7]\n\tv_max_u32 %[t], %[t], %[u]\n\tv_cmp_eq_u32 vcc, -1, %[t]\n\ts_cbranch_vccz " #l "f\n\t" PISS(x)
#define PMOV(x) "v_mov_b64 v[124:125], v[" #x ":" #x "+1]\n\tv_mov_b64 v[126:127], v[" #x "+2:" #x "+3]\n\tv_mov_b64 v[128:129], v[" #x "+4:" #x "+5]\n\tv_mov_b64 v[130:131], v[" #x "+6:" #x "+7]\n\t"
template <int FIRST, int D0> DI u32 poll_data(rsrc_t r, u32 o0, u32 o1, u32x4& v0, u32x4& v1, f32x8& a, f32x8& b, f32x8& c) {
  u32 t, u, n = kSpinCap;
  if (FIRST) asm volatile("" : "={v[132:139]}"(a), "={v[140:147]}"(b), "={v[148:155]}"(c));
  asm volatile("s_sleep %[d0]\n\t"
               PISS(148) "1:\n\t" PCHK(148, 4)
               "s_sub_u32 %[n], %[n], 1\n\ts_cbranch_scc0 1b\n\ts_waitcnt vmcnt(0)\n\t"
               "4:\n\t" PMOV(148) "s_branch 5f\n3:\n\t" PMOV(140) "s_branch 5f\n2:\n\t" PMOV(132) "5:"
               : "=&{v[124:127]}"(v0), "=&{v[128:131]}"(v1), "+{v[132:139]}"(a), "+{v[140:147]}"(b), "+{v[148:155]}"(c), [t] "=&v"(t), [u] "=&v"(u), [n] "+s"(n)
               : [o0] "v"(o0), [o1] "v"(o1), [r] "s"(r), [d] "n"(PSL), [d0] "n"(D0), [w] "n"(2 * PDEP - 2) : "memory", "scc", "vcc");
  return kSpinCap - n;
}
DI u32 poll_data2(rsrc_t r, u32 o0, u32 o1, u32x4& v0, u32x4& v1, f32x8& c) {
  u32 t, u, n = kSpinCap;
  asm volatile("s_waitcnt vmcnt(5)\n1:\n\t"
               "v_max3_u32 %[t], v148, v149, v150\n\tv_max3_u32 %[u], v151, v152, v153\n\tv_max3_u32 %[t], %[t], v154, v155\n\tv_max_u32 %[t], %[t], %[u]\n\t"
               "v_cmp_eq_u32 vcc, -1, %[t]\n\ts_cbranch_vccz 4f\n\ts_sleep %[d]\n\t" PISS(148)
               "s_sub_u32 %[n], %[n], 1\n\ts_waitcnt vmcnt(0)\n\ts_cbranch_scc0 1b\n4:\n\t" PMOV(148)
               : "=&{v[124:127]}"(v0), "=&{v[128:131]}"(v1), "+{v[148:155]}"(c), [t] "=&v"(t), [u] "=&v"(u), [n] "+s"(n)
               : [o0] "v"(o0), [o1] "v"(o1), [r] "s"(r), [d] "n"(PD3) : "memory", "scc", "vcc");
  return kSpinCap - n;
}
DI u32x2 sw16(u32 a, u32 b) { auto r = __builtin_amdgcn_permlane16_swap(a, b, false, false); return (u32x2){r[0], r[1]}; }
DI u32x2 sw32(u32 a, u32 b) { auto r = __builtin_amdgcn_permlane32_swap(a, b, false, false); return (u32x2){r[0], r[1]}; }
DI f32x2 sw16f(float a, float b) { return __builtin_bit_cast(f32x2, sw16(__float_as_uint(a), __float_as_uint(b))); }
template <int C> DI float dpp(float v) { return __uint_as_float(__builtin_amdgcn_update_dpp(0, __float_as_uint(v), C, 15, 15, true)); }
DI float max2f(float a, float b) { return __builtin_elementwise_maximum(a, b); }
DI u32x2 pack(const f32x4& v) { return (u32x2){pk2(v[0], v[1]), pk2(v[2], v[3])}; }
DI u32 act2(f32x2 v) { f32x2 r; const f32x2 k = {0x1p-64f, 0x1p-64f};
  asm("v_pk_mul_f32 %0, %1, %2 clamp" : "=v"(r) : "v"(v), "s"(k)); r *= v; return pk2(r.x, r.y); }
DI u32x2 act(const f32x4& v) { return (u32x2){act2(v.xy), act2(v.zw)}; }

DI void project(const u32x4 (&b)[3], const bf16x8 (&fw)[15], f32x4 (&p)[5]) {
#pragma unroll
  for (int i = 0; i < 5; ++i) p[i] = (f32x4){0.f, 0.f, 0.f, 0.f};
#pragma unroll
  for (int ks = 0; ks < 3; ++ks)
#pragma unroll
    for (int i = 0; i < 5; ++i) p[i] = mfma32(fw[3 * i + ks], as8(b[ks]), p[i]);
}
template <class H> DI void feature_map(const f32x4 (&p)[5], u32 (&ph)[20], H hook) {
  float m = max2f(p[0][0], p[0][1]), n = max2f(-p[0][0], -p[0][1]);
#pragma unroll
  for (int f = 2; f < 20; ++f) { m = max2f(m, p[f >> 2][f & 3]); n = max2f(n, -p[f >> 2][f & 3]); }
  constexpr float L2E = 1.4426950408889634f;
  const f32x2 c = {L2E, -L2E}; f32x2 off = (f32x2){m, n} * -L2E;
  f32x2 e[20], s[2];
#pragma unroll
  for (int f = 0; f < 20; ++f) { const float v = p[f >> 2][f & 3]; const f32x2 t = __builtin_elementwise_fma((f32x2){v, v}, c, off);
    float ex = __builtin_amdgcn_exp2f(t.x); hook(f, ex);
    e[f] = (f32x2){ex, __builtin_amdgcn_exp2f(t.y)};
    s[f & 1] = f < 2 ? e[f] : s[f & 1] + e[f]; }
  s[0] += s[1];
  const f32x2 r = {__builtin_amdgcn_rcpf(s[0].x), __builtin_amdgcn_rcpf(s[0].y)};
#pragma unroll
  for (int f = 0; f < 20; ++f) { const f32x2 t = e[f] * r; ph[f] = pk2(t.x, t.y); }
}
#define SPINS(i, ...) (void)(__VA_ARGS__)
#define STAMP(i) do {} while (0)
#define CSTAMP(i) do {} while (0)
#define STAMP_END do {} while (0)
#define UNROLL_S _Pragma("unroll 1")
#define STAMP_ARG
#define STAMP_PASS
#define AST(i)
#define AST_OUT
#define AST_DECL
#define AST_PUT(b)
DI int lane_id() { u32 l; asm volatile("v_mbcnt_lo_u32_b32 %0, -1, 0\n\tv_mbcnt_hi_u32_b32 %0, -1, %0" : "=v"(l)); __builtin_assume(l < 64); return l; }

DI bf16x8 selector(const int m, const u32 val = 0x3f80u) {
  const u32 one = val << (16 * (m & 1)); const int pos = m >> 1;
  return mk8(pos == 0 ? one : 0, pos == 1 ? one : 0, pos == 2 ? one : 0, pos == 3 ? one : 0);
}

template <int isQ> DI void gemm1(char* __restrict__ ws, const rsrc_t rWS, const u64 tag, const int lane, const int wave, const int mt, const int hq, u32& hf STAMP_ARG) {
    const int g = lane >> 4, c16 = lane & 15, q4 = c16 >> 2, p4 = lane & 3;
    const int th = wave >> 1, ch = wave & 1;
    const u32 trow = 4 * g + q4;
    constexpr u32 RW = isQ ? 160 : 480;
    const u32 rlo = 64 * th + 32 * ch + c16, rhi = 64 * th + 32 * (1 - ch) + c16;
    u32 kl[2], k2[2], hs[2], e1[2];
    const u32 actb = WS_ACT + (2 * mt + th) * ACT_PANEL + 7 * hq * 4096;
#pragma unroll
    for (int i = 0; i < 2; ++i) { const u32 r = (isQ ? L_QI : L_KI) + (i ? rhi : rlo) * RW + 8 * g;
      kl[i] = r + 64 * ch; k2[i] = r + 128 + (isQ ? 0 : 192 * ch);
      hs[i] = actb + (isQ ? ch : 5 + ch) * 4096 + ((i ^ ch) * 2048) + lane * 16;
      e1[i] = actb + 4 * 4096 + ((i ^ ch) * 2048) + (g & 1) * 1024 + (2 * ch + (g >> 1)) * 256 + c16 * 16;
      pin(kl[i]); pin(hs[i]); if (isQ) pin(e1[i]); if (!isQ || !i) pin(k2[i]); }
    hf = lane < 4 ? WS_HF + ((mt * 4 + (hq >> 1)) * 4 + lane) * 128 + (isQ * 2 + (hq & 1)) * 8 : OOB;
    u32 acs = L_CS + (ch * 112 + 4 * g) * 4, ac7 = L_CS + 224 * 4 + g * 16;
    if (isQ) pin(ac7);
    pin(hf); pin(acs);
    f32x32 a0, a1, a2; f32x16 a3; f32x8 a4;
    f32x8 st; f32x32 tb; f32x8 tb2;
    { const u32 xa = SL0 + (64 * th + c16) * 128 + ((g ^ ((c16 >> 1) & 7)) * 16);
      u32 xl0 = xa + ch * 4096, xh0 = xa + (1 - ch) * 4096, xl1 = xl0 ^ 64, xh1 = xh0 ^ 64;
      const u32 w0 = SL0 + W_OFF + trow * 320 + 8 * p4 + 32 * (g & 1), w1 = SL0 + C_OFF + trow * 160 + 8 * p4;
      u32 we = w0 + 64 * ch, wo = we ^ 32, wb = (w0 ^ (32 * ch)) + 128, wc = w1 + 64 * ch, wsh = w1 + 128;
      u32 d0 = SL1 - SL0, d1 = SL2 - SL1, d2 = SL0 - SL2, cnt = 10, tmp;
      const u32 stl = L_ST + rlo * 8, sth = L_ST + rhi * 8;
      STAMP(1); CSTAMP(30);
      __builtin_amdgcn_s_setprio(2);
      if constexpr (isQ) asm volatile(
      "v_mov_b64 v[0:1], 0\n\t"
      "v_mov_b64 v[2:3], 0\n\t"
      "v_mov_b64 v[4:5], 0\n\t"
      "v_mov_b64 v[6:7], 0\n\t"
      "v_mov_b64 v[8:9], 0\n\t"
      "v_mov_b64 v[10:11], 0\n\t"
      "v_mov_b64 v[12:13], 0\n\t"
      "v_mov_b64 v[14:15], 0\n\t"
      "v_mov_b64 v[16:17], 0\n\t"
      "v_mov_b64 v[18:19], 0\n\t"
      "v_mov_b64 v[20:21], 0\n\t"
      "v_mov_b64 v[22:23], 0\n\t"
      "v_mov_b64 v[24:25], 0\n\t"
      "v_mov_b64 v[26:27], 0\n\t"
      "v_mov_b64 v[28:29], 0\n\t"
      "v_mov_b64 v[30:31], 0\n\t"
      "v_mov_b64 v[32:33], 0\n\t"
      "v_mov_b64 v[34:35], 0\n\t"
      "v_mov_b64 v[36:37], 0\n\t"
      "v_mov_b64 v[38:39], 0\n\t"
      "v_mov_b64 v[40:41], 0\n\t"
      "v_mov_b64 v[42:43], 0\n\t"
      "v_mov_b64 v[44:45], 0\n\t"
      "v_mov_b64 v[46:47], 0\n\t"
      "v_mov_b64 v[48:49], 0\n\t"
      "v_mov_b64 v[50:51], 0\n\t"
      "v_mov_b64 v[52:53], 0\n\t"
      "v_mov_b64 v[54:55], 0\n\t"
      "v_mov_b64 v[56:57], 0\n\t"
      "v_mov_b64 v[58:59], 0\n\t"
      "v_mov_b64 v[60:61], 0\n\t"
      "v_mov_b64 v[62:63], 0\n\t"
      "v_mov_b64 v[64:65], 0\n\t"
      "v_mov_b64 v[66:67], 0\n\t"
      "v_mov_b64 v[68:69], 0\n\t"
      "v_mov_b64 v[70:71], 0\n\t"
      "v_mov_b64 v[72:73], 0\n\t"
      "v_mov_b64 v[74:75], 0\n\t"
      "v_mov_b64 v[76:77], 0\n\t"
      "v_mov_b64 v[78:79], 0\n\t"
      "v_mov_b64 v[80:81], 0\n\t"
      "v_mov_b64 v[82:83], 0\n\t"
      "v_mov_b64 v[84:85], 0\n\t"
      "v_mov_b64 v[86:87], 0\n\t"
      "v_mov_b64 v[88:89], 0\n\t"
      "v_mov_b64 v[90:91], 0\n\t"
      "v_mov_b64 v[92:93], 0\n\t"
      "v_mov_b64 v[94:95], 0\n\t"
      "v_mov_b64 v[96:97], 0\n\t"
      "v_mov_b64 v[98:99], 0\n\t"
      "v_mov_b64 v[100:101], 0\n\t"
      "v_mov_b64 v[102:103], 0\n\t"
      "v_mov_b64 v[104:105], 0\n\t"
      "v_mov_b64 v[106:107], 0\n\t"
      "v_mov_b64 v[108:109], 0\n\t"
      "v_mov_b64 v[110:111], 0\n\t"
      "v_mov_b64 v[112:113], 0\n\t"
      "v_mov_b64 v[114:115], 0\n\t"
      "v_mov_b64 v[116:117], 0\n\t"
      "v_mov_b64 v[118:119], 0\n\t"
      "s_waitcnt lgkmcnt(0)\n\t"
      "s_barrier\n\t"
      "ds_read_b128 v[120:123], %[xl0] offset:0\n\t"
      "ds_read_b128 v[124:127], %[xl0] offset:2048\n\t"
      "ds_read_b64_tr_b16 v[136:137], %[we] offset:0\n\t"
      "ds_read_b64_tr_b16 v[138:139], %[we] offset:5120\n\t"
      "ds_read_b128 v[128:131], %[xh0] offset:0\n\t"
      "ds_read_b128 v[132:135], %[xh0] offset:2048\n\t"
      "ds_read_b64_tr_b16 v[140:141], %[wo] offset:0\n\t"
      "ds_read_b64_tr_b16 v[142:143], %[wo] offset:5120\n\t"
      "ds_read_b64_tr_b16 v[144:145], %[wb] offset:0\n\t"
      "ds_read_b64_tr_b16 v[146:147], %[wb] offset:5120\n\t"
      "ds_read_b64_tr_b16 v[148:149], %[we] offset:192\n\t"
      "ds_read_b64_tr_b16 v[150:151], %[we] offset:5312\n\t"
      "ds_read_b64_tr_b16 v[152:153], %[wo] offset:192\n\t"
      "ds_read_b64_tr_b16 v[154:155], %[wo] offset:5312\n\t"
      "ds_read_b64_tr_b16 v[156:157], %[wc] offset:0\n\t"
      "ds_read_b64_tr_b16 v[158:159], %[wc] offset:2560\n\t"
      "ds_read_b64_tr_b16 v[160:161], %[wc] offset:32\n\t"
      "ds_read_b64_tr_b16 v[162:163], %[wc] offset:2592\n\t"
      "ds_read_b64_tr_b16 v[164:165], %[ws] offset:0\n\t"
      "ds_read_b64_tr_b16 v[166:167], %[ws] offset:2560\n\t"
      "s_waitcnt lgkmcnt(0)\n\t"
      "1:\n\t"
      "v_mfma_f32_16x16x32_bf16 v[112:115], v[164:167], v[120:123], v[112:115]\n\t"
      "ds_read_b64_tr_b16 v[212:213], %[ws] offset:5120\n\t"
      "v_mfma_f32_16x16x32_bf16 v[116:119], v[164:167], v[124:127], v[116:119]\n\t"
      "ds_read_b64_tr_b16 v[214:215], %[ws] offset:7680\n\t"
      "v_mfma_f32_16x16x32_bf16 v[0:3], v[136:139], v[120:123], v[0:3]\n\t"
      "ds_read_b128 v[168:171], %[xl1] offset:0\n\t"
      "v_mfma_f32_16x16x32_bf16 v[4:7], v[136:139], v[124:127], v[4:7]\n\t"
      "ds_read_b128 v[172:175], %[xl1] offset:2048\n\t"
      "v_mfma_f32_16x16x32_bf16 v[8:11], v[136:139], v[128:131], v[8:11]\n\t"
      "ds_read_b64_tr_b16 v[184:185], %[we] offset:10240\n\t"
      "v_mfma_f32_16x16x32_bf16 v[12:15], v[136:139], v[132:135], v[12:15]\n\t"
      "ds_read_b64_tr_b16 v[186:187], %[we] offset:15360\n\t"
      "v_mfma_f32_16x16x32_bf16 v[28:31], v[140:143], v[132:135], v[28:31]\n\t"
      "ds_read_b128 v[176:179], %[xh1] offset:0\n\t"
      "v_mfma_f32_16x16x32_bf16 v[24:27], v[140:143], v[128:131], v[24:27]\n\t"
      "ds_read_b128 v[180:183], %[xh1] offset:2048\n\t"
      "v_mfma_f32_16x16x32_bf16 v[20:23], v[140:143], v[124:127], v[20:23]\n\t"
      "ds_read_b64_tr_b16 v[188:189], %[wo] offset:10240\n\tv_add_u32 %[xl0], %[d0], %[xl0]\n\t"
      "v_mfma_f32_16x16x32_bf16 v[16:19], v[140:143], v[120:123], v[16:19]\n\t"
      "ds_read_b64_tr_b16 v[190:191], %[wo] offset:15360\n\tv_add_u32 %[xh0], %[d0], %[xh0]\n\t"
      "v_mfma_f32_16x16x32_bf16 v[32:35], v[144:147], v[120:123], v[32:35]\n\t"
      "ds_read_b64_tr_b16 v[192:193], %[wb] offset:10240\n\tv_add_u32 %[ws], %[d0], %[ws]\n\t"
      "v_mfma_f32_16x16x32_bf16 v[36:39], v[144:147], v[124:127], v[36:39]\n\t"
      "ds_read_b64_tr_b16 v[194:195], %[wb] offset:15360\n\t"
      "v_mfma_f32_16x16x32_bf16 v[40:43], v[144:147], v[128:131], v[40:43]\n\t"
      "ds_read_b64_tr_b16 v[196:197], %[we] offset:10432\n\t"
      "v_mfma_f32_16x16x32_bf16 v[44:47], v[144:147], v[132:135], v[44:47]\n\t"
      "ds_read_b64_tr_b16 v[198:199], %[we] offset:15552\n\t"
      "v_mfma_f32_16x16x32_bf16 v[60:63], v[148:151], v[132:135], v[60:63]\n\t"
      "ds_read_b64_tr_b16 v[200:201], %[wo] offset:10432\n\t"
      "v_mfma_f32_16x16x32_bf16 v[56:59], v[148:151], v[128:131], v[56:59]\n\t"
      "ds_read_b64_tr_b16 v[202:203], %[wo] offset:15552\n\t"
      "v_mfma_f32_16x16x32_bf16 v[52:55], v[148:151], v[124:127], v[52:55]\n\t"
      "ds_read_b64_tr_b16 v[204:205], %[wc] offset:5120\n\t"
      "v_mfma_f32_16x16x32_bf16 v[48:51], v[148:151], v[120:123], v[48:51]\n\t"
      "ds_read_b64_tr_b16 v[206:207], %[wc] offset:7680\n\t"
      "v_mfma_f32_16x16x32_bf16 v[64:67], v[152:155], v[120:123], v[64:67]\n\t"
      "ds_read_b64_tr_b16 v[208:209], %[wc] offset:5152\n\t"
      "v_mfma_f32_16x16x32_bf16 v[68:71], v[152:155], v[124:127], v[68:71]\n\t"
      "ds_read_b64_tr_b16 v[210:211], %[wc] offset:7712\n\t"
      "v_mfma_f32_16x16x32_bf16 v[72:75], v[152:155], v[128:131], v[72:75]\n\t"
      "v_add_u32 %[we], %[d0], %[we]\n\t"
      "v_mfma_f32_16x16x32_bf16 v[76:79], v[152:155], v[132:135], v[76:79]\n\t"
      "v_add_u32 %[wo], %[d0], %[wo]\n\t"
      "v_mfma_f32_16x16x32_bf16 v[92:95], v[156:159], v[132:135], v[92:95]\n\t"
      "v_add_u32 %[wb], %[d0], %[wb]\n\t"
      "v_mfma_f32_16x16x32_bf16 v[88:91], v[156:159], v[128:131], v[88:91]\n\t"
      "v_add_u32 %[wc], %[d0], %[wc]\n\t"
      "v_mfma_f32_16x16x32_bf16 v[84:87], v[156:159], v[124:127], v[84:87]\n\t"
      "s_sub_u32 %[cnt], %[cnt], 1\n\t"
      "v_mfma_f32_16x16x32_bf16 v[80:83], v[156:159], v[120:123], v[80:83]\n\t"
      "s_mov_b32 %[tmp], %[d0]\n\t"
      "v_mfma_f32_16x16x32_bf16 v[96:99], v[160:163], v[120:123], v[96:99]\n\t"
      "s_mov_b32 %[d0], %[d1]\n\t"
      "v_mfma_f32_16x16x32_bf16 v[100:103], v[160:163], v[124:127], v[100:103]\n\t"
      "s_mov_b32 %[d1], %[d2]\n\t"
      "v_mfma_f32_16x16x32_bf16 v[104:107], v[160:163], v[128:131], v[104:107]\n\t"
      "s_mov_b32 %[d2], %[tmp]\n\t"
      "v_mfma_f32_16x16x32_bf16 v[108:111], v[160:163], v[132:135], v[108:111]\n\t"
      "s_cmp_eq_u32 %[cnt], 0\n\t"
      "s_waitcnt vmcnt(0) lgkmcnt(0)\n\t"
      "s_barrier\n\t"
      "s_cbranch_scc1 2f\n\t"
      "v_mfma_f32_16x16x32_bf16 v[112:115], v[212:215], v[168:171], v[112:115]\n\t"
      "ds_read_b64_tr_b16 v[164:165], %[ws] offset:0\n\t"
      "v_mfma_f32_16x16x32_bf16 v[116:119], v[212:215], v[172:175], v[116:119]\n\t"
      "ds_read_b64_tr_b16 v[166:167], %[ws] offset:2560\n\t"
      "v_mfma_f32_16x16x32_bf16 v[0:3], v[184:187], v[168:171], v[0:3]\n\t"
      "ds_read_b128 v[120:123], %[xl0] offset:0\n\t"
      "v_mfma_f32_16x16x32_bf16 v[4:7], v[184:187], v[172:175], v[4:7]\n\t"
      "ds_read_b128 v[124:127], %[xl0] offset:2048\n\t"
      "v_mfma_f32_16x16x32_bf16 v[8:11], v[184:187], v[176:179], v[8:11]\n\t"
      "ds_read_b64_tr_b16 v[136:137], %[we] offset:0\n\t"
      "v_mfma_f32_16x16x32_bf16 v[12:15], v[184:187], v[180:183], v[12:15]\n\t"
      "ds_read_b64_tr_b16 v[138:139], %[we] offset:5120\n\t"
      "v_mfma_f32_16x16x32_bf16 v[28:31], v[188:191], v[180:183], v[28:31]\n\t"
      "ds_read_b128 v[128:131], %[xh0] offset:0\n\t"
      "v_mfma_f32_16x16x32_bf16 v[24:27], v[188:191], v[176:179], v[24:27]\n\t"
      "ds_read_b128 v[132:135], %[xh0] offset:2048\n\t"
      "v_mfma_f32_16x16x32_bf16 v[20:23], v[188:191], v[172:175], v[20:23]\n\t"
      "ds_read_b64_tr_b16 v[140:141], %[wo] offset:0\n\t"
      "v_mfma_f32_16x16x32_bf16 v[16:19], v[188:191], v[168:171], v[16:19]\n\t"
      "ds_read_b64_tr_b16 v[142:143], %[wo] offset:5120\n\t"
      "v_mfma_f32_16x16x32_bf16 v[32:35], v[192:195], v[168:171], v[32:35]\n\t"
      "ds_read_b64_tr_b16 v[144:145], %[wb] offset:0\n\t"
      "v_mfma_f32_16x16x32_bf16 v[36:39], v[192:195], v[172:175], v[36:39]\n\t"
      "ds_read_b64_tr_b16 v[146:147], %[wb] offset:5120\n\t"
      "v_mfma_f32_16x16x32_bf16 v[40:43], v[192:195], v[176:179], v[40:43]\n\t"
      "ds_read_b64_tr_b16 v[148:149], %[we] offset:192\n\t"
      "v_mfma_f32_16x16x32_bf16 v[44:47], v[192:195], v[180:183], v[44:47]\n\t"
      "ds_read_b64_tr_b16 v[150:151], %[we] offset:5312\n\t"
      "v_mfma_f32_16x16x32_bf16 v[60:63], v[196:199], v[180:183], v[60:63]\n\t"
      "ds_read_b64_tr_b16 v[152:153], %[wo] offset:192\n\t"
      "v_mfma_f32_16x16x32_bf16 v[56:59], v[196:199], v[176:179], v[56:59]\n\t"
      "ds_read_b64_tr_b16 v[154:155], %[wo] offset:5312\n\t"
      "v_mfma_f32_16x16x32_bf16 v[52:55], v[196:199], v[172:175], v[52:55]\n\t"
      "ds_read_b64_tr_b16 v[156:157], %[wc] offset:0\n\t"
      "v_mfma_f32_16x16x32_bf16 v[48:51], v[196:199], v[168:171], v[48:51]\n\t"
      "ds_read_b64_tr_b16 v[158:159], %[wc] offset:2560\n\t"
      "v_mfma_f32_16x16x32_bf16 v[64:67], v[200:203], v[168:171], v[64:67]\n\t"
      "ds_read_b64_tr_b16 v[160:161], %[wc] offset:32\n\t"
      "v_mfma_f32_16x16x32_bf16 v[68:71], v[200:203], v[172:175], v[68:71]\n\t"
      "ds_read_b64_tr_b16 v[162:163], %[wc] offset:2592\n\t"
      "v_mfma_f32_16x16x32_bf16 v[72:75], v[200:203], v[176:179], v[72:75]\n\t"
      "v_add_u32 %[xl1], %[d2], %[xl1]\n\t"
      "v_mfma_f32_16x16x32_bf16 v[76:79], v[200:203], v[180:183], v[76:79]\n\t"
      "v_add_u32 %[xh1], %[d2], %[xh1]\n\t"
      "v_mfma_f32_16x16x32_bf16 v[92:95], v[204:207], v[180:183], v[92:95]\n\t"
      "v_mfma_f32_16x16x32_bf16 v[88:91], v[204:207], v[176:179], v[88:91]\n\t"
      "v_mfma_f32_16x16x32_bf16 v[84:87], v[204:207], v[172:175], v[84:87]\n\t"
      "v_mfma_f32_16x16x32_bf16 v[80:83], v[204:207], v[168:171], v[80:83]\n\t"
      "v_mfma_f32_16x16x32_bf16 v[96:99], v[208:211], v[168:171], v[96:99]\n\t"
      "v_mfma_f32_16x16x32_bf16 v[100:103], v[208:211], v[172:175], v[100:103]\n\t"
      "v_mfma_f32_16x16x32_bf16 v[104:107], v[208:211], v[176:179], v[104:107]\n\t"
      "v_mfma_f32_16x16x32_bf16 v[108:111], v[208:211], v[180:183], v[108:111]\n\t"
      "s_waitcnt lgkmcnt(0)\n\t"
      "s_branch 1b\n\t"
      "2:\n\t"
      "v_mfma_f32_16x16x32_bf16 v[112:115], v[212:215], v[168:171], v[112:115]\n\t"
      "v_mfma_f32_16x16x32_bf16 v[116:119], v[212:215], v[172:175], v[116:119]\n\t"
      "v_mfma_f32_16x16x32_bf16 v[0:3], v[184:187], v[168:171], v[0:3]\n\t"
      "ds_read_b64 v[120:121], %[stl] offset:0\n\t"
      "v_mfma_f32_16x16x32_bf16 v[4:7], v[184:187], v[172:175], v[4:7]\n\t"
      "ds_read_b64 v[122:123], %[stl] offset:128\n\t"
      "v_mfma_f32_16x16x32_bf16 v[8:11], v[184:187], v[176:179], v[8:11]\n\t"
      "ds_read_b64 v[124:125], %[sth] offset:0\n\t"
      "v_mfma_f32_16x16x32_bf16 v[12:15], v[184:187], v[180:183], v[12:15]\n\t"
      "ds_read_b64 v[126:127], %[sth] offset:128\n\t"
      "v_mfma_f32_16x16x32_bf16 v[28:31], v[188:191], v[180:183], v[28:31]\n\t"
      "ds_read_b128 v[128:131], %[acs] offset:0\n\t"
      "v_mfma_f32_16x16x32_bf16 v[24:27], v[188:191], v[176:179], v[24:27]\n\t"
      "ds_read_b128 v[132:135], %[acs] offset:64\n\t"
      "v_mfma_f32_16x16x32_bf16 v[20:23], v[188:191], v[172:175], v[20:23]\n\t"
      "ds_read_b128 v[136:139], %[acs] offset:128\n\t"
      "v_mfma_f32_16x16x32_bf16 v[16:19], v[188:191], v[168:171], v[16:19]\n\t"
      "ds_read_b128 v[140:143], %[acs] offset:192\n\t"
      "v_mfma_f32_16x16x32_bf16 v[32:35], v[192:195], v[168:171], v[32:35]\n\t"
      "ds_read_b128 v[144:147], %[acs] offset:256\n\t"
      "v_mfma_f32_16x16x32_bf16 v[36:39], v[192:195], v[172:175], v[36:39]\n\t"
      "ds_read_b128 v[148:151], %[acs] offset:1024\n\t"
      "v_mfma_f32_16x16x32_bf16 v[40:43], v[192:195], v[176:179], v[40:43]\n\t"
      "ds_read_b128 v[152:155], %[acs] offset:1088\n\t"
      "v_mfma_f32_16x16x32_bf16 v[44:47], v[192:195], v[180:183], v[44:47]\n\t"
      "ds_read_b128 v[156:159], %[acs] offset:1152\n\t"
      "v_mfma_f32_16x16x32_bf16 v[60:63], v[196:199], v[180:183], v[60:63]\n\t"
      "ds_read_b128 v[160:163], %[acs] offset:1216\n\t"
      "v_mfma_f32_16x16x32_bf16 v[56:59], v[196:199], v[176:179], v[56:59]\n\t"
      "ds_read_b128 v[164:167], %[acs] offset:1280\n\t"
      "v_mfma_f32_16x16x32_bf16 v[52:55], v[196:199], v[172:175], v[52:55]\n\t"
      "v_mfma_f32_16x16x32_bf16 v[48:51], v[196:199], v[168:171], v[48:51]\n\t"
      "v_mfma_f32_16x16x32_bf16 v[64:67], v[200:203], v[168:171], v[64:67]\n\t"
      "v_mfma_f32_16x16x32_bf16 v[68:71], v[200:203], v[172:175], v[68:71]\n\t"
      "v_mfma_f32_16x16x32_bf16 v[72:75], v[200:203], v[176:179], v[72:75]\n\t"
      "v_mfma_f32_16x16x32_bf16 v[76:79], v[200:203], v[180:183], v[76:79]\n\t"
      "v_mfma_f32_16x16x32_bf16 v[92:95], v[204:207], v[180:183], v[92:95]\n\t"
      "v_mfma_f32_16x16x32_bf16 v[88:91], v[204:207], v[176:179], v[88:91]\n\t"
      "v_mfma_f32_16x16x32_bf16 v[84:87], v[204:207], v[172:175], v[84:87]\n\t"
      "v_mfma_f32_16x16x32_bf16 v[80:83], v[204:207], v[168:171], v[80:83]\n\t"
      "v_mfma_f32_16x16x32_bf16 v[96:99], v[208:211], v[168:171], v[96:99]\n\t"
      "v_mfma_f32_16x16x32_bf16 v[100:103], v[208:211], v[172:175], v[100:103]\n\t"
      "v_mfma_f32_16x16x32_bf16 v[104:107], v[208:211], v[176:179], v[104:107]\n\t"
      "v_mfma_f32_16x16x32_bf16 v[108:111], v[208:211], v[180:183], v[108:111]\n\t"
      "s_waitcnt vmcnt(0) lgkmcnt(0)\n\t"
      "s_nop 7\n\t"
        : "=&{v[0:31]}"(a0), "=&{v[32:63]}"(a1), "=&{v[64:95]}"(a2), "=&{v[96:111]}"(a3), "=&{v[112:119]}"(a4),
          "=&{v[120:127]}"(st), "=&{v[128:159]}"(tb), "=&{v[160:167]}"(tb2),
          [xl0] "+v"(xl0), [xh0] "+v"(xh0), [xl1] "+v"(xl1), [xh1] "+v"(xh1), [we] "+v"(we), [wo] "+v"(wo), [wb] "+v"(wb), [wc] "+v"(wc), [ws] "+v"(wsh),
          [d0] "+s"(d0), [d1] "+s"(d1), [d2] "+s"(d2), [cnt] "+s"(cnt), [tmp] "=&s"(tmp)
        : [stl] "v"(stl), [sth] "v"(sth), [acs] "v"(acs) : "memory", "scc", "v168", "v169", "v170", "v171", "v172", "v173", "v174", "v175", "v176", "v177", "v178", "v179", "v180", "v181", "v182", "v183", "v184", "v185", "v186", "v187", "v188", "v189", "v190", "v191", "v192", "v193", "v194", "v195", "v196", "v197", "v198", "v199", "v200", "v201", "v202", "v203", "v204", "v205", "v206", "v207", "v208", "v209", "v210", "v211", "v212", "v213", "v214", "v215");
      else asm volatile(
      "v_mov_b64 v[0:1], 0\n\t"
      "v_mov_b64 v[2:3], 0\n\t"
      "v_mov_b64 v[4:5], 0\n\t"
      "v_mov_b64 v[6:7], 0\n\t"
      "v_mov_b64 v[8:9], 0\n\t"
      "v_mov_b64 v[10:11], 0\n\t"
      "v_mov_b64 v[12:13], 0\n\t"
      "v_mov_b64 v[14:15], 0\n\t"
      "v_mov_b64 v[16:17], 0\n\t"
      "v_mov_b64 v[18:19], 0\n\t"
      "v_mov_b64 v[20:21], 0\n\t"
      "v_mov_b64 v[22:23], 0\n\t"
      "v_mov_b64 v[24:25], 0\n\t"
      "v_mov_b64 v[26:27], 0\n\t"
      "v_mov_b64 v[28:29], 0\n\t"
      "v_mov_b64 v[30:31], 0\n\t"
      "v_mov_b64 v[32:33], 0\n\t"
      "v_mov_b64 v[34:35], 0\n\t"
      "v_mov_b64 v[36:37], 0\n\t"
      "v_mov_b64 v[38:39], 0\n\t"
      "v_mov_b64 v[40:41], 0\n\t"
      "v_mov_b64 v[42:43], 0\n\t"
      "v_mov_b64 v[44:45], 0\n\t"
      "v_mov_b64 v[46:47], 0\n\t"
      "v_mov_b64 v[48:49], 0\n\t"
      "v_mov_b64 v[50:51], 0\n\t"
      "v_mov_b64 v[52:53], 0\n\t"
      "v_mov_b64 v[54:55], 0\n\t"
      "v_mov_b64 v[56:57], 0\n\t"
      "v_mov_b64 v[58:59], 0\n\t"
      "v_mov_b64 v[60:61], 0\n\t"
      "v_mov_b64 v[62:63], 0\n\t"
      "v_mov_b64 v[64:65], 0\n\t"
      "v_mov_b64 v[66:67], 0\n\t"
      "v_mov_b64 v[68:69], 0\n\t"
      "v_mov_b64 v[70:71], 0\n\t"
      "v_mov_b64 v[72:73], 0\n\t"
      "v_mov_b64 v[74:75], 0\n\t"
      "v_mov_b64 v[76:77], 0\n\t"
      "v_mov_b64 v[78:79], 0\n\t"
      "v_mov_b64 v[80:81], 0\n\t"
      "v_mov_b64 v[82:83], 0\n\t"
      "v_mov_b64 v[84:85], 0\n\t"
      "v_mov_b64 v[86:87], 0\n\t"
      "v_mov_b64 v[88:89], 0\n\t"
      "v_mov_b64 v[90:91], 0\n\t"
      "v_mov_b64 v[92:93], 0\n\t"
      "v_mov_b64 v[94:95], 0\n\t"
      "v_mov_b64 v[96:97], 0\n\t"
      "v_mov_b64 v[98:99], 0\n\t"
      "v_mov_b64 v[100:101], 0\n\t"
      "v_mov_b64 v[102:103], 0\n\t"
      "v_mov_b64 v[104:105], 0\n\t"
      "v_mov_b64 v[106:107], 0\n\t"
      "v_mov_b64 v[108:109], 0\n\t"
      "v_mov_b64 v[110:111], 0\n\t"
      "s_waitcnt lgkmcnt(0)\n\t"
      "s_barrier\n\t"
      "ds_read_b128 v[120:123], %[xl0] offset:0\n\t"
      "ds_read_b128 v[124:127], %[xl0] offset:2048\n\t"
      "ds_read_b64_tr_b16 v[136:137], %[we] offset:0\n\t"
      "ds_read_b64_tr_b16 v[138:139], %[we] offset:5120\n\t"
      "ds_read_b128 v[128:131], %[xh0] offset:0\n\t"
      "ds_read_b128 v[132:135], %[xh0] offset:2048\n\t"
      "ds_read_b64_tr_b16 v[140:141], %[wo] offset:0\n\t"
      "ds_read_b64_tr_b16 v[142:143], %[wo] offset:5120\n\t"
      "ds_read_b64_tr_b16 v[144:145], %[wb] offset:0\n\t"
      "ds_read_b64_tr_b16 v[146:147], %[wb] offset:5120\n\t"
      "ds_read_b64_tr_b16 v[148:149], %[we] offset:192\n\t"
      "ds_read_b64_tr_b16 v[150:151], %[we] offset:5312\n\t"
      "ds_read_b64_tr_b16 v[152:153], %[wo] offset:192\n\t"
      "ds_read_b64_tr_b16 v[154:155], %[wo] offset:5312\n\t"
      "ds_read_b64_tr_b16 v[156:157], %[wc] offset:0\n\t"
      "ds_read_b64_tr_b16 v[158:159], %[wc] offset:2560\n\t"
      "ds_read_b64_tr_b16 v[160:161], %[wc] offset:32\n\t"
      "ds_read_b64_tr_b16 v[162:163], %[wc] offset:2592\n\t"
      "s_waitcnt lgkmcnt(0)\n\t"
      "1:\n\t"
      "v_mfma_f32_16x16x32_bf16 v[0:3], v[136:139], v[120:123], v[0:3]\n\t"
      "ds_read_b128 v[168:171], %[xl1] offset:0\n\t"
      "v_mfma_f32_16x16x32_bf16 v[4:7], v[136:139], v[124:127], v[4:7]\n\t"
      "ds_read_b128 v[172:175], %[xl1] offset:2048\n\t"
      "v_mfma_f32_16x16x32_bf16 v[8:11], v[136:139], v[128:131], v[8:11]\n\t"
      "ds_read_b64_tr_b16 v[184:185], %[we] offset:10240\n\t"
      "v_mfma_f32_16x16x32_bf16 v[12:15], v[136:139], v[132:135], v[12:15]\n\t"
      "ds_read_b64_tr_b16 v[186:187], %[we] offset:15360\n\t"
      "v_mfma_f32_16x16x32_bf16 v[28:31], v[140:143], v[132:135], v[28:31]\n\t"
      "ds_read_b128 v[176:179], %[xh1] offset:0\n\t"
      "v_mfma_f32_16x16x32_bf16 v[24:27], v[140:143], v[128:131], v[24:27]\n\t"
      "ds_read_b128 v[180:183], %[xh1] offset:2048\n\t"
      "v_mfma_f32_16x16x32_bf16 v[20:23], v[140:143], v[124:127], v[20:23]\n\t"
      "ds_read_b64_tr_b16 v[188:189], %[wo] offset:10240\n\tv_add_u32 %[xl0], %[d0], %[xl0]\n\t"
      "v_mfma_f32_16x16x32_bf16 v[16:19], v[140:143], v[120:123], v[16:19]\n\t"
      "ds_read_b64_tr_b16 v[190:191], %[wo] offset:15360\n\tv_add_u32 %[xh0], %[d0], %[xh0]\n\t"
      "v_mfma_f32_16x16x32_bf16 v[32:35], v[144:147], v[120:123], v[32:35]\n\t"
      "ds_read_b64_tr_b16 v[192:193], %[wb] offset:10240\n\t"
      "v_mfma_f32_16x16x32_bf16 v[36:39], v[144:147], v[124:127], v[36:39]\n\t"
      "ds_read_b64_tr_b16 v[194:195], %[wb] offset:15360\n\t"
      "v_mfma_f32_16x16x32_bf16 v[40:43], v[144:147], v[128:131], v[40:43]\n\t"
      "ds_read_b64_tr_b16 v[196:197], %[we] offset:10432\n\t"
      "v_mfma_f32_16x16x32_bf16 v[44:47], v[144:147], v[132:135], v[44:47]\n\t"
      "ds_read_b64_tr_b16 v[198:199], %[we] offset:15552\n\t"
      "v_mfma_f32_16x16x32_bf16 v[60:63], v[148:151], v[132:135], v[60:63]\n\t"
      "ds_read_b64_tr_b16 v[200:201], %[wo] offset:10432\n\t"
      "v_mfma_f32_16x16x32_bf16 v[56:59], v[148:151], v[128:131], v[56:59]\n\t"
      "ds_read_b64_tr_b16 v[202:203], %[wo] offset:15552\n\t"
      "v_mfma_f32_16x16x32_bf16 v[52:55], v[148:151], v[124:127], v[52:55]\n\t"
      "ds_read_b64_tr_b16 v[204:205], %[wc] offset:5120\n\t"
      "v_mfma_f32_16x16x32_bf16 v[48:51], v[148:151], v[120:123], v[48:51]\n\t"
      "ds_read_b64_tr_b16 v[206:207], %[wc] offset:7680\n\t"
      "v_mfma_f32_16x16x32_bf16 v[64:67], v[152:155], v[120:123], v[64:67]\n\t"
      "ds_read_b64_tr_b16 v[208:209], %[wc] offset:5152\n\t"
      "v_mfma_f32_16x16x32_bf16 v[68:71], v[152:155], v[124:127], v[68:71]\n\t"
      "ds_read_b64_tr_b16 v[210:211], %[wc] offset:7712\n\t"
      "v_mfma_f32_16x16x32_bf16 v[72:75], v[152:155], v[128:131], v[72:75]\n\t"
      "v_add_u32 %[we], %[d0], %[we]\n\t"
      "v_mfma_f32_16x16x32_bf16 v[76:79], v[152:155], v[132:135], v[76:79]\n\t"
      "v_add_u32 %[wo], %[d0], %[wo]\n\t"
      "v_mfma_f32_16x16x32_bf16 v[92:95], v[156:159], v[132:135], v[92:95]\n\t"
      "v_add_u32 %[wb], %[d0], %[wb]\n\t"
      "v_mfma_f32_16x16x32_bf16 v[88:91], v[156:159], v[128:131], v[88:91]\n\t"
      "v_add_u32 %[wc], %[d0], %[wc]\n\t"
      "v_mfma_f32_16x16x32_bf16 v[84:87], v[156:159], v[124:127], v[84:87]\n\t"
      "s_sub_u32 %[cnt], %[cnt], 1\n\t"
      "v_mfma_f32_16x16x32_bf16 v[80:83], v[156:159], v[120:123], v[80:83]\n\t"
      "s_mov_b32 %[tmp], %[d0]\n\t"
      "v_mfma_f32_16x16x32_bf16 v[96:99], v[160:163], v[120:123], v[96:99]\n\t"
      "s_mov_b32 %[d0], %[d1]\n\t"
      "v_mfma_f32_16x16x32_bf16 v[100:103], v[160:163], v[124:127], v[100:103]\n\t"
      "s_mov_b32 %[d1], %[d2]\n\t"
      "v_mfma_f32_16x16x32_bf16 v[104:107], v[160:163], v[128:131], v[104:107]\n\t"
      "s_mov_b32 %[d2], %[tmp]\n\t"
      "v_mfma_f32_16x16x32_bf16 v[108:111], v[160:163], v[132:135], v[108:111]\n\t"
      "s_cmp_eq_u32 %[cnt], 0\n\t"
      "s_waitcnt vmcnt(0) lgkmcnt(0)\n\t"
      "s_barrier\n\t"
      "s_cbranch_scc1 2f\n\t"
      "v_mfma_f32_16x16x32_bf16 v[0:3], v[184:187], v[168:171], v[0:3]\n\t"
      "ds_read_b128 v[120:123], %[xl0] offset:0\n\t"
      "v_mfma_f32_16x16x32_bf16 v[4:7], v[184:187], v[172:175], v[4:7]\n\t"
      "ds_read_b128 v[124:127], %[xl0] offset:2048\n\t"
      "v_mfma_f32_16x16x32_bf16 v[8:11], v[184:187], v[176:179], v[8:11]\n\t"
      "ds_read_b64_tr_b16 v[136:137], %[we] offset:0\n\t"
      "v_mfma_f32_16x16x32_bf16 v[12:15], v[184:187], v[180:183], v[12:15]\n\t"
      "ds_read_b64_tr_b16 v[138:139], %[we] offset:5120\n\t"
      "v_mfma_f32_16x16x32_bf16 v[28:31], v[188:191], v[180:183], v[28:31]\n\t"
      "ds_read_b128 v[128:131], %[xh0] offset:0\n\t"
      "v_mfma_f32_16x16x32_bf16 v[24:27], v[188:191], v[176:179], v[24:27]\n\t"
      "ds_read_b128 v[132:135], %[xh0] offset:2048\n\t"
      "v_mfma_f32_16x16x32_bf16 v[20:23], v[188:191], v[172:175], v[20:23]\n\t"
      "ds_read_b64_tr_b16 v[140:141], %[wo] offset:0\n\t"
      "v_mfma_f32_16x16x32_bf16 v[16:19], v[188:191], v[168:171], v[16:19]\n\t"
      "ds_read_b64_tr_b16 v[142:143], %[wo] offset:5120\n\t"
      "v_mfma_f32_16x16x32_bf16 v[32:35], v[192:195], v[168:171], v[32:35]\n\t"
      "ds_read_b64_tr_b16 v[144:145], %[wb] offset:0\n\t"
      "v_mfma_f32_16x16x32_bf16 v[36:39], v[192:195], v[172:175], v[36:39]\n\t"
      "ds_read_b64_tr_b16 v[146:147], %[wb] offset:5120\n\t"
      "v_mfma_f32_16x16x32_bf16 v[40:43], v[192:195], v[176:179], v[40:43]\n\t"
      "ds_read_b64_tr_b16 v[148:149], %[we] offset:192\n\t"
      "v_mfma_f32_16x16x32_bf16 v[44:47], v[192:195], v[180:183], v[44:47]\n\t"
      "ds_read_b64_tr_b16 v[150:151], %[we] offset:5312\n\t"
      "v_mfma_f32_16x16x32_bf16 v[60:63], v[196:199], v[180:183], v[60:63]\n\t"
      "ds_read_b64_tr_b16 v[152:153], %[wo] offset:192\n\t"
      "v_mfma_f32_16x16x32_bf16 v[56:59], v[196:199], v[176:179], v[56:59]\n\t"
      "ds_read_b64_tr_b16 v[154:155], %[wo] offset:5312\n\t"
      "v_mfma_f32_16x16x32_bf16 v[52:55], v[196:199], v[172:175], v[52:55]\n\t"
      "ds_read_b64_tr_b16 v[156:157], %[wc] offset:0\n\t"
      "v_mfma_f32_16x16x32_bf16 v[48:51], v[196:199], v[168:171], v[48:51]\n\t"
      "ds_read_b64_tr_b16 v[158:159], %[wc] offset:2560\n\t"
      "v_mfma_f32_16x16x32_bf16 v[64:67], v[200:203], v[168:171], v[64:67]\n\t"
      "ds_read_b64_tr_b16 v[160:161], %[wc] offset:32\n\t"
      "v_mfma_f32_16x16x32_bf16 v[68:71], v[200:203], v[172:175], v[68:71]\n\t"
      "ds_read_b64_tr_b16 v[162:163], %[wc] offset:2592\n\t"
      "v_mfma_f32_16x16x32_bf16 v[72:75], v[200:203], v[176:179], v[72:75]\n\t"
      "v_add_u32 %[xl1], %[d2], %[xl1]\n\t"
      "v_mfma_f32_16x16x32_bf16 v[76:79], v[200:203], v[180:183], v[76:79]\n\t"
      "v_add_u32 %[xh1], %[d2], %[xh1]\n\t"
      "v_mfma_f32_16x16x32_bf16 v[92:95], v[204:207], v[180:183], v[92:95]\n\t"
      "v_mfma_f32_16x16x32_bf16 v[88:91], v[204:207], v[176:179], v[88:91]\n\t"
      "v_mfma_f32_16x16x32_bf16 v[84:87], v[204:207], v[172:175], v[84:87]\n\t"
      "v_mfma_f32_16x16x32_bf16 v[80:83], v[204:207], v[168:171], v[80:83]\n\t"
      "v_mfma_f32_16x16x32_bf16 v[96:99], v[208:211], v[168:171], v[96:99]\n\t"
      "v_mfma_f32_16x16x32_bf16 v[100:103], v[208:211], v[172:175], v[100:103]\n\t"
      "v_mfma_f32_16x16x32_bf16 v[104:107], v[208:211], v[176:179], v[104:107]\n\t"
      "v_mfma_f32_16x16x32_bf16 v[108:111], v[208:211], v[180:183], v[108:111]\n\t"
      "s_waitcnt lgkmcnt(0)\n\t"
      "s_branch 1b\n\t"
      "2:\n\t"
      "v_mfma_f32_16x16x32_bf16 v[0:3], v[184:187], v[168:171], v[0:3]\n\t"
      "ds_read_b64 v[120:121], %[stl] offset:0\n\t"
      "v_mfma_f32_16x16x32_bf16 v[4:7], v[184:187], v[172:175], v[4:7]\n\t"
      "ds_read_b64 v[122:123], %[stl] offset:128\n\t"
      "v_mfma_f32_16x16x32_bf16 v[8:11], v[184:187], v[176:179], v[8:11]\n\t"
      "ds_read_b64 v[124:125], %[sth] offset:0\n\t"
      "v_mfma_f32_16x16x32_bf16 v[12:15], v[184:187], v[180:183], v[12:15]\n\t"
      "ds_read_b64 v[126:127], %[sth] offset:128\n\t"
      "v_mfma_f32_16x16x32_bf16 v[28:31], v[188:191], v[180:183], v[28:31]\n\t"
      "ds_read_b128 v[128:131], %[acs] offset:0\n\t"
      "v_mfma_f32_16x16x32_bf16 v[24:27], v[188:191], v[176:179], v[24:27]\n\t"
      "ds_read_b128 v[132:135], %[acs] offset:64\n\t"
      "v_mfma_f32_16x16x32_bf16 v[20:23], v[188:191], v[172:175], v[20:23]\n\t"
      "ds_read_b128 v[136:139], %[acs] offset:128\n\t"
      "v_mfma_f32_16x16x32_bf16 v[16:19], v[188:191], v[168:171], v[16:19]\n\t"
      "ds_read_b128 v[140:143], %[acs] offset:192\n\t"
      "v_mfma_f32_16x16x32_bf16 v[32:35], v[192:195], v[168:171], v[32:35]\n\t"
      "ds_read_b128 v[144:147], %[acs] offset:256\n\t"
      "v_mfma_f32_16x16x32_bf16 v[36:39], v[192:195], v[172:175], v[36:39]\n\t"
      "ds_read_b128 v[148:151], %[acs] offset:320\n\t"
      "v_mfma_f32_16x16x32_bf16 v[40:43], v[192:195], v[176:179], v[40:43]\n\t"
      "ds_read_b128 v[152:155], %[acs] offset:384\n\t"
      "v_mfma_f32_16x16x32_bf16 v[44:47], v[192:195], v[180:183], v[44:47]\n\t"
      "ds_read_b128 v[156:159], %[acs] offset:1344\n\t"
      "v_mfma_f32_16x16x32_bf16 v[60:63], v[196:199], v[180:183], v[60:63]\n\t"
      "ds_read_b128 v[160:163], %[acs] offset:1408\n\t"
      "v_mfma_f32_16x16x32_bf16 v[56:59], v[196:199], v[176:179], v[56:59]\n\t"
      "v_mfma_f32_16x16x32_bf16 v[52:55], v[196:199], v[172:175], v[52:55]\n\t"
      "v_mfma_f32_16x16x32_bf16 v[48:51], v[196:199], v[168:171], v[48:51]\n\t"
      "v_mfma_f32_16x16x32_bf16 v[64:67], v[200:203], v[168:171], v[64:67]\n\t"
      "v_mfma_f32_16x16x32_bf16 v[68:71], v[200:203], v[172:175], v[68:71]\n\t"
      "v_mfma_f32_16x16x32_bf16 v[72:75], v[200:203], v[176:179], v[72:75]\n\t"
      "v_mfma_f32_16x16x32_bf16 v[76:79], v[200:203], v[180:183], v[76:79]\n\t"
      "v_mfma_f32_16x16x32_bf16 v[92:95], v[204:207], v[180:183], v[92:95]\n\t"
      "v_mfma_f32_16x16x32_bf16 v[88:91], v[204:207], v[176:179], v[88:91]\n\t"
      "v_mfma_f32_16x16x32_bf16 v[84:87], v[204:207], v[172:175], v[84:87]\n\t"
      "v_mfma_f32_16x16x32_bf16 v[80:83], v[204:207], v[168:171], v[80:83]\n\t"
      "v_mfma_f32_16x16x32_bf16 v[96:99], v[208:211], v[168:171], v[96:99]\n\t"
      "v_mfma_f32_16x16x32_bf16 v[100:103], v[208:211], v[172:175], v[100:103]\n\t"
      "v_mfma_f32_16x16x32_bf16 v[104:107], v[208:211], v[176:179], v[104:107]\n\t"
      "v_mfma_f32_16x16x32_bf16 v[108:111], v[208:211], v[180:183], v[108:111]\n\t"
      "s_waitcnt vmcnt(0) lgkmcnt(0)\n\t"
      "s_nop 7\n\t"
        : "=&{v[0:31]}"(a0), "=&{v[32:63]}"(a1), "=&{v[64:95]}"(a2), "=&{v[96:111]}"(a3),
          "=&{v[120:127]}"(st), "=&{v[128:159]}"(tb), "=&{v[160:167]}"(tb2),
          [xl0] "+v"(xl0), [xh0] "+v"(xh0), [xl1] "+v"(xl1), [xh1] "+v"(xh1), [we] "+v"(we), [wo] "+v"(wo), [wb] "+v"(wb), [wc] "+v"(wc),
          [d0] "+s"(d0), [d1] "+s"(d1), [d2] "+s"(d2), [cnt] "+s"(cnt), [tmp] "=&s"(tmp)
        : [stl] "v"(stl), [sth] "v"(sth), [acs] "v"(acs) : "memory", "scc", "v168", "v169", "v170", "v171", "v172", "v173", "v174", "v175", "v176", "v177", "v178", "v179", "v180", "v181", "v182", "v183", "v184", "v185", "v186", "v187", "v188", "v189", "v190", "v191", "v192", "v193", "v194", "v195", "v196", "v197", "v198", "v199", "v200", "v201", "v202", "v203", "v204", "v205", "v206", "v207", "v208", "v209", "v210", "v211", "v212", "v213", "v214", "v215");
    }
    CSTAMP(31); STAMP(2);
    auto acc = [&](int t, int j) { const int b = t < 7 ? (t * 4 + j) * 4 : 112 + 4 * j; f32x4 v;
#pragma unroll
      for (int e = 0; e < 4; ++e) { const int i = b + e; v[e] = i < 32 ? a0[i] : i < 64 ? a1[i - 32] : i < 96 ? a2[i - 64] : i < 112 ? a3[i - 96] : a4[i - 112]; }
      return v; };
    auto tbl = [&](int i) { return i < 8 ? (f32x4){tb[4 * i], tb[4 * i + 1], tb[4 * i + 2], tb[4 * i + 3]} : (f32x4){tb2[4 * i - 32], tb2[4 * i - 31], tb2[4 * i - 30], tb2[4 * i - 29]}; };
    f32x4 cs[8], bb[8];
    if (isQ) {
#pragma unroll
      for (int t = 0; t < 5; ++t) { cs[t] = tbl(t); bb[t] = tbl(5 + t); }
      cs[5] = ldsf4(acs + 320); cs[6] = ldsf4(acs + 384); cs[7] = ldsf4(ac7);
    } else {
#pragma unroll
      for (int t = 0; t < 7; ++t) cs[t] = tbl(t);
      bb[5] = tbl(7); bb[6] = tbl(8); }
    auto norm = [&](int t, int j, bool bias) {
      const f32x4 c = bias ? cs[t] * st[2 * j + 1] + bb[t] : cs[t] * st[2 * j + 1];
      return acc(t, j) * st[2 * j] + c; };
    auto hstore = [&](int t, u32 o) {
#pragma unroll
      for (int j = 0; j < 4; ++j) { const u32x2 u = act(norm(t, j, true)), v = act(norm(t + 1, j, true));
        st128((u32x4){u.x, u.y, v.x, v.y}, rWS, hs[j >> 1] + o + (j & 1) * 1024); } };
    auto lstore = [&](int t, u32 o) {
#pragma unroll
      for (int j = 0; j < 4; ++j) { const u32 p = kl[j >> 1] + (j & 1) * 16 * RW + o;
        sts64(p, pack(norm(t, j, false))); sts64(p + 32, pack(norm(t + 1, j, false))); } };
    __builtin_amdgcn_s_setprio(3);
    if constexpr (!isQ) {
      lstore(0, 0);
#pragma unroll
      for (int j = 0; j < 4; ++j) sts64(k2[j >> 1] + (j & 1) * 16 * RW, pack(norm(2, j, false)));
      lbar();
      STAMP(3);
      if (HF) { if (hq < 7) hstore(5, 0);
        if (HF == 1) publish<0>(C_H, rWS, hf, tag); }
      __builtin_amdgcn_s_setprio(MPR);
      lstore(3, 352);
      lds_arrive(C_V);
      STAMP(4);
      if (!HF && hq < 7) hstore(5, 0);
    } else {
      hstore(0, 0); hstore(3, 8192);
#pragma unroll
      for (int i = 0; i < 2; ++i) { const u32x2 u = act(norm(2, 2 * i, true)), v = act(norm(2, 2 * i + 1, true));
        const u32x2 sx = sw16(u.x, v.x), sy = sw16(u.y, v.y);
        st128((u32x4){sx.x, sy.x, sx.y, sy.y}, rWS, e1[i]); }
      STAMP(3);
      lstore(5, 0);
#pragma unroll
      for (int j = 0; j < 2; ++j) sts64(k2[0] + j * 16 * RW, pack(norm(7, j, false)));
      lbar();
    }
    STAMP(5);
}

DI void tail(const u16* __restrict__ x, char* __restrict__ ws, u16* __restrict__ out, const u64 tag, const int wave,
             const u32 va, const u32 pw, const f32x32& r0, const f32x32& r1, const f32x16& r2 STAMP_ARG) {
    int lane = lane_id(), g = lane >> 4, c16 = lane & 15;
    const int q4 = c16 >> 2, p4 = lane & 3;
    const int bid = blockIdx.x, xcd = bid & 7, idx = bid >> 3;
    const int mt = (xcd & 1) * 8 + (idx >> 2), kh = (xcd >> 1) & 1, n2 = 2 * (idx & 3) + (xcd >> 2);
    const rsrc_t rWS = mkrsrc(ws, WS_END), rX = mkrsrc(x, S_ * D_ * 2);
    const u32 me = (((mt * 2 + kh) * 8 + n2) * 4 + wave);
    auto acc = [&](int i) { return i < 32 ? r0[i] : i < 64 ? r1[i - 32] : r2[i - 64]; };
    const u32 l16 = lane * 16, l8 = lane * 8;
#pragma unroll
    for (int s = 0; s < 1; ++s) {
      const int jj = (s + 1) & 1, hh = (s + 1) >> 1, b = 40 * jj + 20 * hh; u32 w[10];
      const u32 so = WS_XCH + (me ^ (32 * jj) ^ (4 * hh)) * 7680;
#pragma unroll
      for (int i = 0; i < 10; ++i) w[i] = pk2(acc(b + 2 * i), acc(b + 2 * i + 1));
      __builtin_amdgcn_raw_buffer_store_b128((u32x4){w[0], w[1], w[2], w[3]}, rWS, l16, so + s * 2048, 16);
      __builtin_amdgcn_raw_buffer_store_b128((u32x4){w[4], w[5], w[6], w[7]}, rWS, l16 + 1024, so + s * 2048, 16);
      __builtin_amdgcn_raw_buffer_store_b64((u32x2){w[8], w[9]}, rWS, l8, so + 6144 + s * 512, 16); }
    asm volatile("" ::: "memory");
    const u32 ro = (u32)(mt * 128 + kh * 64 + 16 * wave + c16) * 1280 + n2 * 160;
    f32x16 s0; f32x4 s1;
    { u32x4 xr[3];
#pragma unroll
      for (int t = 0; t < 3; ++t) xr[t] = __builtin_amdgcn_raw_buffer_load_b128(rX, t < 2 || g < 2 ? ro + 16 * g + 64 * t : OOB, 0, 0);
      f32x4 v[5];
#pragma unroll
      for (int t = 0; t < 5; ++t) v[t] = (f32x4){r0[4 * t], r0[4 * t + 1], r0[4 * t + 2], r0[4 * t + 3]} * 0x1p64f + ldsf4(L_B2 + 64 * t + 16 * g);
      const bf16x8 x0 = selector(c16 - 8 * g), x1 = selector(c16 + 16 - 8 * g);
      u32 xf = lane < 3 ? WS_OF + ((me >> 2) ^ (8 * ((lane + 1) & 1)) ^ ((lane + 1) >> 1)) * 128 + 64 + lane * 8 : OOB; pin(xf);
      publish<3>(C_X, rWS, xf, tag);
#pragma unroll
      for (int t = 0; t < 5; ++t) { v[t] = mfma32(t & 1 ? x1 : x0, as8(xr[t >> 1]), v[t]);
#pragma unroll
        for (int e = 0; e < 4; ++e) if (t < 4) s0[4 * t + e] = v[t][e]; else s1[e] = v[t][e]; } }
    STAMP(8);
    u32 o1 = ro + 32 * (g & 1) + 16 * (g >> 1), o2 = ro + 128 + 8 * g; pin(o1); pin(o2);
    { const u32 sb = WS_ACT + (2 * mt + kh) * ACT_PANEL + 54 * 4096, ro4 = OR ? idx & 3 : 0;
      u32 so = sb + ro4 * 20480, wc = (20 * g + q4) * 160 + 8 * p4 + ro4 * 25600, wd = (40 * g + q4) * 160 + 8 * p4 + ro4 * 25600, cnt, c2, late, dl[4];
#pragma unroll
      for (int k = 0; k < 4; ++k) dl[k] = (k + ro4) & 3 ? 25600 : -76800;
      const u32 fo = lane < 8 && HN ? OOB : lane < 8 ? sb + 16384 + (lane >> 1) * 20480 + wave * 1024 + (lane & 1) * 512 : WS_OF + (me >> 2) * 128 + (lane < 16 ? lane * 8 - 64 : 64 + (lane & 3) * 8);
      const u32 xi = WS_XCH + me * 7680 + l16, xj = WS_XCH + me * 7680 + 6144 + l8;
      const u64 ex = lane < 8 ? ~0ull : tag;
      const bf16x8 e0 = selector(q4 == g ? p4 : 8, 0x5f80u), e1 = selector(q4 == g ? 4 + p4 : 8, 0x5f80u);
      AST_DECL
      asm volatile(
      AST(0)
      "s_mov_b32 %[cnt], 0x100000\n\t"
      "4:\n\t"
      "ds_read_b32 v238, %[pw] offset:0\n\t"
      "s_sub_u32 %[cnt], %[cnt], 1\n\t"
      "s_cbranch_scc1 5f\n\t"
      "s_waitcnt lgkmcnt(0)\n\t"
      "v_cmp_gt_u32 vcc, 3, v238\n\t"
      "s_cbranch_vccnz 4b\n\t"
      "5:\n\t"
      "s_mov_b32 %[late], 7\n\t"
      "s_mov_b32 %[cnt], 0x40000\n\t"
      "s_sleep 12\n\t"
      "7:\n\t"
      "buffer_load_dwordx2 v[130:131], %[fa], %[rA], 0 offen sc1\n\t"
      "6:\n\t"
      "s_waitcnt vmcnt(0)\n\t"
      "v_cmp_eq_u64 vcc, %[ex], v[130:131]\n\t"
      "s_xor_b32 vcc_lo, vcc_lo, 0xff00\n\t"
      "s_and_b32 vcc_lo, vcc_lo, 0xffff\n\t"
      "s_cbranch_scc0 8f\n\t"
      "s_sleep 4\n\t"
      "s_or_b32 vcc_lo, vcc_lo, 0xff00\n\t"
      "s_mov_b32 vcc_hi, 7\n\t"
      "v_cndmask_b32 v238, 2.0, %[fa], vcc\n\t"
      "buffer_load_dwordx2 v[130:131], v238, %[rA], 0 offen sc1\n\t"
      "s_sub_u32 %[cnt], %[cnt], 1\n\t"
      "s_cbranch_scc0 6b\n\t"
      "s_mov_b32 %[cnt], 0\n\t"
      "8:\n\t"
      AST(1)
      "s_mov_b64 vcc, 0\n\t"
      "s_mov_b32 vcc_hi, 7\n\t"
      "v_cndmask_b32 v239, 2.0, %[fa], vcc\n\t"
      "buffer_load_dwordx4 v[58:61], %[va], %[rA], %[so] offen sc1\n\t"
      "s_add_u32 %[so], %[so], 0x1000\n\t"
      "buffer_load_dwordx2 v[130:131], v239, %[rA], 0 offen sc1\n\t"
      "buffer_load_dwordx4 v[62:65], %[va], %[rA], %[so] offen sc1\n\t"
      "s_add_u32 %[so], %[so], 0x1000\n\t"
      "buffer_load_dwordx4 v[66:69], %[va], %[rA], %[so] offen sc1\n\t"
      "s_add_u32 %[so], %[so], 0x1000\n\t"
      "buffer_load_dwordx4 v[70:73], %[va], %[rA], %[so] offen sc1\n\t"
      "s_add_u32 %[so], %[so], 0x1000\n\t"
      "buffer_load_dwordx4 v[74:77], %[va], %[rA], %[so] offen sc1\n\t"
      "s_add_u32 %[so], %[so], 0x1000\n\t"
      "s_cmp_eq_u32 %[so], %[se]\n\t"
      "s_cselect_b32 %[so], %[sb], %[so]\n\t"
      "buffer_load_dwordx4 v[78:81], %[va], %[rA], %[so] offen sc1\n\t"
      "s_add_u32 %[so], %[so], 0x1000\n\t"
      "buffer_load_dwordx4 v[82:85], %[va], %[rA], %[so] offen sc1\n\t"
      "s_add_u32 %[so], %[so], 0x1000\n\t"
      "buffer_load_dwordx4 v[86:89], %[va], %[rA], %[so] offen sc1\n\t"
      "s_add_u32 %[so], %[so], 0x1000\n\t"
      "buffer_load_dwordx4 v[90:93], %[va], %[rA], %[so] offen sc1\n\t"
      "s_add_u32 %[so], %[so], 0x1000\n\t"
      "buffer_load_dwordx4 v[94:97], %[va], %[rA], %[so] offen sc1\n\t"
      "s_add_u32 %[so], %[so], 0x1000\n\t"
      "s_cmp_eq_u32 %[so], %[se]\n\t"
      "s_cselect_b32 %[so], %[sb], %[so]\n\t"
      "buffer_load_dwordx4 v[98:101], %[va], %[rA], %[so] offen sc1\n\t"
      "s_add_u32 %[so], %[so], 0x1000\n\t"
      "buffer_load_dwordx4 v[102:105], %[va], %[rA], %[so] offen sc1\n\t"
      "s_add_u32 %[so], %[so], 0x1000\n\t"
      "s_mov_b32 %[c2], %[cnt]\n\t"
      "s_mov_b32 %[cnt], 0x100000\n\t"
      "4:\n\t"
      "ds_read_b32 v238, %[pw] offset:0\n\t"
      "s_sub_u32 %[cnt], %[cnt], 1\n\t"
      "s_cbranch_scc1 5f\n\t"
      "s_waitcnt lgkmcnt(0)\n\t"
      "v_cmp_gt_u32 vcc, 4, v238\n\t"
      "s_cbranch_vccnz 4b\n\t"
      "5:\n\t"
      "s_mov_b32 %[cnt], %[c2]\n\t"
      AST(2)
      "ds_read_b64_tr_b16 v[138:139], %[wc] offset:0\n\t"
      "ds_read_b64_tr_b16 v[140:141], %[wc] offset:640\n\t"
      "ds_read_b64_tr_b16 v[142:143], %[wc] offset:32\n\t"
      "ds_read_b64_tr_b16 v[144:145], %[wc] offset:672\n\t"
      "ds_read_b64_tr_b16 v[146:147], %[wc] offset:64\n\t"
      "ds_read_b64_tr_b16 v[148:149], %[wc] offset:704\n\t"
      "ds_read_b64_tr_b16 v[150:151], %[wc] offset:96\n\t"
      "ds_read_b64_tr_b16 v[152:153], %[wc] offset:736\n\t"
      "ds_read_b64_tr_b16 v[154:155], %[wc] offset:128\n\t"
      "ds_read_b64_tr_b16 v[156:157], %[wc] offset:768\n\t"
      "ds_read_b64_tr_b16 v[158:159], %[wc] offset:1280\n\t"
      "ds_read_b64_tr_b16 v[160:161], %[wc] offset:1920\n\t"
      "ds_read_b64_tr_b16 v[162:163], %[wc] offset:1312\n\t"
      "ds_read_b64_tr_b16 v[164:165], %[wc] offset:1952\n\t"
      "ds_read_b64_tr_b16 v[166:167], %[wc] offset:1344\n\t"
      "ds_read_b64_tr_b16 v[168:169], %[wc] offset:1984\n\t"
      "ds_read_b64_tr_b16 v[170:171], %[wc] offset:1376\n\t"
      "ds_read_b64_tr_b16 v[172:173], %[wc] offset:2016\n\t"
      "ds_read_b64_tr_b16 v[174:175], %[wc] offset:1408\n\t"
      "ds_read_b64_tr_b16 v[176:177], %[wc] offset:2048\n\t"
      "ds_read_b64_tr_b16 v[178:179], %[wc] offset:12800\n\t"
      "ds_read_b64_tr_b16 v[180:181], %[wc] offset:13440\n\t"
      "ds_read_b64_tr_b16 v[182:183], %[wc] offset:12832\n\t"
      "ds_read_b64_tr_b16 v[184:185], %[wc] offset:13472\n\t"
      "ds_read_b64_tr_b16 v[186:187], %[wc] offset:12864\n\t"
      "ds_read_b64_tr_b16 v[188:189], %[wc] offset:13504\n\t"
      "ds_read_b64_tr_b16 v[190:191], %[wc] offset:12896\n\t"
      "ds_read_b64_tr_b16 v[192:193], %[wc] offset:13536\n\t"
      "ds_read_b64_tr_b16 v[194:195], %[wc] offset:12928\n\t"
      "ds_read_b64_tr_b16 v[196:197], %[wc] offset:13568\n\t"
      "ds_read_b64_tr_b16 v[198:199], %[wc] offset:14080\n\t"
      "ds_read_b64_tr_b16 v[200:201], %[wc] offset:14720\n\t"
      "ds_read_b64_tr_b16 v[202:203], %[wc] offset:14112\n\t"
      "ds_read_b64_tr_b16 v[204:205], %[wc] offset:14752\n\t"
      "ds_read_b64_tr_b16 v[206:207], %[wc] offset:14144\n\t"
      "ds_read_b64_tr_b16 v[208:209], %[wc] offset:14784\n\t"
      "ds_read_b64_tr_b16 v[210:211], %[wc] offset:14176\n\t"
      "ds_read_b64_tr_b16 v[212:213], %[wc] offset:14816\n\t"
      "ds_read_b64_tr_b16 v[214:215], %[wc] offset:14208\n\t"
      "ds_read_b64_tr_b16 v[216:217], %[wc] offset:14848\n\t"
      "s_waitcnt vmcnt(12) lgkmcnt(15)\n\t"
      AST(3)
      "v_mfma_f32_16x16x32_bf16 v[218:221], v[138:141], v[58:61], v[0:3]\n\t"
      "buffer_load_dwordx4 v[106:109], %[va], %[rA], %[so] offen sc1\n\t"
      "s_add_u32 %[so], %[so], 0x1000\n\t"
      "v_mfma_f32_16x16x32_bf16 v[222:225], v[142:145], v[58:61], v[4:7]\n\t"
      "buffer_load_dwordx4 v[110:113], %[va], %[rA], %[so] offen sc1\n\t"
      "s_add_u32 %[so], %[so], 0x1000\n\t"
      "v_mfma_f32_16x16x32_bf16 v[226:229], v[146:149], v[58:61], v[8:11]\n\t"
      "v_max3_u32 v238, v58, v59, v58\n\t"
      "v_max3_u32 v239, v60, v61, v58\n\t"
      "v_mfma_f32_16x16x32_bf16 v[230:233], v[150:153], v[58:61], v[12:15]\n\t"
      "v_mfma_f32_16x16x32_bf16 v[234:237], v[154:157], v[58:61], v[16:19]\n\t"
      "s_waitcnt vmcnt(12) lgkmcnt(15)\n\t"
      "v_cmp_eq_u64 vcc, %[ex], v[130:131]\n\t"
      "s_movk_i32 %[c2], 0x1000\n\t"
      "s_andn2_b32 %[late], 7, vcc_hi\n\t"
      "v_mfma_f32_16x16x32_bf16 v[218:221], v[158:161], v[62:65], v[218:221]\n\t"
      "buffer_load_dwordx4 v[114:117], %[va], %[rA], %[so] offen sc1\n\t"
      "s_add_u32 %[so], %[so], 0x1000\n\t"
      "s_cmp_eq_u32 %[so], %[se]\n\t"
      "s_cselect_b32 %[so], %[sb], %[so]\n\t"
      "v_mfma_f32_16x16x32_bf16 v[222:225], v[162:165], v[62:65], v[222:225]\n\t"
      "buffer_load_dwordx4 v[118:121], %[va], %[rA], %[so] offen sc1\n\t"
      "s_add_u32 %[so], %[so], 0x1000\n\t"
      "v_max3_u32 v238, v62, v63, v238\n\t"
      "v_max3_u32 v239, v64, v65, v239\n\t"
      "v_mfma_f32_16x16x32_bf16 v[226:229], v[166:169], v[62:65], v[226:229]\n\t"
      "ds_read_b64_tr_b16 v[138:139], %[wd] offset:2560\n\t"
      "ds_read_b64_tr_b16 v[140:141], %[wd] offset:5760\n\t"
      "ds_read_b64_tr_b16 v[142:143], %[wd] offset:2592\n\t"
      "ds_read_b64_tr_b16 v[144:145], %[wd] offset:5792\n\t"
      "v_mfma_f32_16x16x32_bf16 v[230:233], v[170:173], v[62:65], v[230:233]\n\t"
      "ds_read_b64_tr_b16 v[146:147], %[wd] offset:2624\n\t"
      "ds_read_b64_tr_b16 v[148:149], %[wd] offset:5824\n\t"
      "ds_read_b64_tr_b16 v[150:151], %[wd] offset:2656\n\t"
      "ds_read_b64_tr_b16 v[152:153], %[wd] offset:5856\n\t"
      "v_mfma_f32_16x16x32_bf16 v[234:237], v[174:177], v[62:65], v[234:237]\n\t"
      "ds_read_b64_tr_b16 v[154:155], %[wd] offset:2688\n\t"
      "ds_read_b64_tr_b16 v[156:157], %[wd] offset:5888\n\t"
      "s_waitcnt vmcnt(13) lgkmcnt(15)\n\t"
      "v_mfma_f32_16x16x32_bf16 v[218:221], v[178:181], v[66:69], v[218:221]\n\t"
      "buffer_load_dwordx4 v[122:125], %[va], %[rA], %[so] offen sc1\n\t"
      "s_add_u32 %[so], %[so], 0x1000\n\t"
      "buffer_load_dwordx4 v[126:129], %[va], %[rA], %[so] offen sc1\n\t"
      "s_add_u32 %[so], %[so], 0x1000\n\t"
      "v_mfma_f32_16x16x32_bf16 v[222:225], v[182:185], v[66:69], v[222:225]\n\t"
      "v_max3_u32 v238, v66, v67, v238\n\t"
      "v_max3_u32 v239, v68, v69, v239\n\t"
      "v_add_u32 %[wc], %[d1], %[wc]\n\t"
      "v_add_u32 %[wd], %[d1], %[wd]\n\t"
      "v_mfma_f32_16x16x32_bf16 v[226:229], v[186:189], v[66:69], v[226:229]\n\t"
      "ds_read_b64_tr_b16 v[158:159], %[wc] offset:0\n\t"
      "ds_read_b64_tr_b16 v[160:161], %[wc] offset:640\n\t"
      "ds_read_b64_tr_b16 v[162:163], %[wc] offset:32\n\t"
      "ds_read_b64_tr_b16 v[164:165], %[wc] offset:672\n\t"
      "v_mfma_f32_16x16x32_bf16 v[230:233], v[190:193], v[66:69], v[230:233]\n\t"
      "ds_read_b64_tr_b16 v[166:167], %[wc] offset:64\n\t"
      "ds_read_b64_tr_b16 v[168:169], %[wc] offset:704\n\t"
      "ds_read_b64_tr_b16 v[170:171], %[wc] offset:96\n\t"
      "ds_read_b64_tr_b16 v[172:173], %[wc] offset:736\n\t"
      "v_mfma_f32_16x16x32_bf16 v[234:237], v[194:197], v[66:69], v[234:237]\n\t"
      "ds_read_b64_tr_b16 v[174:175], %[wc] offset:128\n\t"
      "ds_read_b64_tr_b16 v[176:177], %[wc] offset:768\n\t"
      "s_waitcnt vmcnt(14) lgkmcnt(15)\n\t"
      "v_mfma_f32_16x16x32_bf16 v[218:221], v[198:201], v[70:73], v[218:221]\n\t"
      "buffer_load_dwordx4 v[130:133], %[va], %[rA], %[so] offen sc1\n\t"
      "s_add_u32 %[so], %[so], 0x1000\n\t"
      "buffer_load_dwordx4 v[134:137], %[va], %[rA], %[so] offen sc1\n\t"
      "s_add_u32 %[so], %[so], 0x1000\n\t"
      "v_mfma_f32_16x16x32_bf16 v[222:225], v[202:205], v[70:73], v[222:225]\n\t"
      "s_cmp_eq_u32 %[so], %[se]\n\t"
      "s_cselect_b32 %[so], %[sb], %[so]\n\t"
      "v_max3_u32 v238, v70, v71, v238\n\t"
      "v_max3_u32 v239, v72, v73, v239\n\t"
      "v_mfma_f32_16x16x32_bf16 v[226:229], v[206:209], v[70:73], v[226:229]\n\t"
      "ds_read_b64_tr_b16 v[178:179], %[wc] offset:1280\n\t"
      "ds_read_b64_tr_b16 v[180:181], %[wc] offset:1920\n\t"
      "ds_read_b64_tr_b16 v[182:183], %[wc] offset:1312\n\t"
      "ds_read_b64_tr_b16 v[184:185], %[wc] offset:1952\n\t"
      "v_mfma_f32_16x16x32_bf16 v[230:233], v[210:213], v[70:73], v[230:233]\n\t"
      "ds_read_b64_tr_b16 v[186:187], %[wc] offset:1344\n\t"
      "ds_read_b64_tr_b16 v[188:189], %[wc] offset:1984\n\t"
      "ds_read_b64_tr_b16 v[190:191], %[wc] offset:1376\n\t"
      "ds_read_b64_tr_b16 v[192:193], %[wc] offset:2016\n\t"
      "v_mfma_f32_16x16x32_bf16 v[234:237], v[214:217], v[70:73], v[234:237]\n\t"
      "ds_read_b64_tr_b16 v[194:195], %[wc] offset:1408\n\t"
      "ds_read_b64_tr_b16 v[196:197], %[wc] offset:2048\n\t"
      "s_waitcnt vmcnt(15) lgkmcnt(15)\n\t"
      "v_mfma_f32_16x16x32_bf16 v[218:221], v[138:141], v[74:77], v[218:221]\n\t"
      "buffer_load_dwordx4 v[20:23], %[xi], %[rA], 0 offen offset:0 sc1\n\t"
      "buffer_load_dwordx4 v[24:27], %[xi], %[rA], 0 offen offset:1024 sc1\n\t"
      "v_max3_u32 v238, v74, v75, v238\n\t"
      "v_mfma_f32_16x16x32_bf16 v[222:225], v[142:145], v[74:77], v[222:225]\n\t"
      "v_max3_u32 v239, v76, v77, v239\n\t"
      "ds_read_b64_tr_b16 v[198:199], %[wc] offset:12800\n\t"
      "ds_read_b64_tr_b16 v[200:201], %[wc] offset:13440\n\t"
      "v_mfma_f32_16x16x32_bf16 v[226:229], v[146:149], v[74:77], v[226:229]\n\t"
      "ds_read_b64_tr_b16 v[202:203], %[wc] offset:12832\n\t"
      "ds_read_b64_tr_b16 v[204:205], %[wc] offset:13472\n\t"
      "ds_read_b64_tr_b16 v[206:207], %[wc] offset:12864\n\t"
      "v_mfma_f32_16x16x32_bf16 v[230:233], v[150:153], v[74:77], v[230:233]\n\t"
      "ds_read_b64_tr_b16 v[208:209], %[wc] offset:13504\n\t"
      "ds_read_b64_tr_b16 v[210:211], %[wc] offset:12896\n\t"
      "ds_read_b64_tr_b16 v[212:213], %[wc] offset:13536\n\t"
      "v_mfma_f32_16x16x32_bf16 v[234:237], v[154:157], v[74:77], v[234:237]\n\t"
      "ds_read_b64_tr_b16 v[214:215], %[wc] offset:12928\n\t"
      "ds_read_b64_tr_b16 v[216:217], %[wc] offset:13568\n\t"
      "s_waitcnt vmcnt(16) lgkmcnt(15)\n\t"
      "v_mfma_f32_16x16x32_bf16 v[218:221], v[158:161], v[78:81], v[218:221]\n\t"
      "buffer_load_dwordx4 v[28:31], %[xi], %[rA], 0 offen offset:2048 sc1\n\t"
      "buffer_load_dwordx4 v[32:35], %[xi], %[rA], 0 offen offset:3072 sc1\n\t"
      "v_max3_u32 v238, v78, v79, v238\n\t"
      "v_mfma_f32_16x16x32_bf16 v[222:225], v[162:165], v[78:81], v[222:225]\n\t"
      "v_max3_u32 v239, v80, v81, v239\n\t"
      "ds_read_b64_tr_b16 v[138:139], %[wc] offset:14080\n\t"
      "ds_read_b64_tr_b16 v[140:141], %[wc] offset:14720\n\t"
      "v_mfma_f32_16x16x32_bf16 v[226:229], v[166:169], v[78:81], v[226:229]\n\t"
      "ds_read_b64_tr_b16 v[142:143], %[wc] offset:14112\n\t"
      "ds_read_b64_tr_b16 v[144:145], %[wc] offset:14752\n\t"
      "ds_read_b64_tr_b16 v[146:147], %[wc] offset:14144\n\t"
      "v_mfma_f32_16x16x32_bf16 v[230:233], v[170:173], v[78:81], v[230:233]\n\t"
      "ds_read_b64_tr_b16 v[148:149], %[wc] offset:14784\n\t"
      "ds_read_b64_tr_b16 v[150:151], %[wc] offset:14176\n\t"
      "ds_read_b64_tr_b16 v[152:153], %[wc] offset:14816\n\t"
      "v_mfma_f32_16x16x32_bf16 v[234:237], v[174:177], v[78:81], v[234:237]\n\t"
      "ds_read_b64_tr_b16 v[154:155], %[wc] offset:14208\n\t"
      "ds_read_b64_tr_b16 v[156:157], %[wc] offset:14848\n\t"
      "s_waitcnt vmcnt(17) lgkmcnt(15)\n\t"
      "v_mfma_f32_16x16x32_bf16 v[218:221], v[178:181], v[82:85], v[218:221]\n\t"
      "buffer_load_dwordx4 v[36:39], %[xi], %[rA], %[c2] offen offset:0 sc1\n\t"
      "buffer_load_dwordx4 v[40:43], %[xi], %[rA], %[c2] offen offset:1024 sc1\n\t"
      "v_max3_u32 v238, v82, v83, v238\n\t"
      "v_mfma_f32_16x16x32_bf16 v[222:225], v[182:185], v[82:85], v[222:225]\n\t"
      "v_max3_u32 v239, v84, v85, v239\n\t"
      "ds_read_b64_tr_b16 v[158:159], %[wd] offset:2560\n\t"
      "ds_read_b64_tr_b16 v[160:161], %[wd] offset:5760\n\t"
      "v_mfma_f32_16x16x32_bf16 v[226:229], v[186:189], v[82:85], v[226:229]\n\t"
      "ds_read_b64_tr_b16 v[162:163], %[wd] offset:2592\n\t"
      "ds_read_b64_tr_b16 v[164:165], %[wd] offset:5792\n\t"
      "ds_read_b64_tr_b16 v[166:167], %[wd] offset:2624\n\t"
      "v_mfma_f32_16x16x32_bf16 v[230:233], v[190:193], v[82:85], v[230:233]\n\t"
      "ds_read_b64_tr_b16 v[168:169], %[wd] offset:5824\n\t"
      "ds_read_b64_tr_b16 v[170:171], %[wd] offset:2656\n\t"
      "ds_read_b64_tr_b16 v[172:173], %[wd] offset:5856\n\t"
      "v_mfma_f32_16x16x32_bf16 v[234:237], v[194:197], v[82:85], v[234:237]\n\t"
      "ds_read_b64_tr_b16 v[174:175], %[wd] offset:2688\n\t"
      "ds_read_b64_tr_b16 v[176:177], %[wd] offset:5888\n\t"
      "s_waitcnt vmcnt(18) lgkmcnt(15)\n\t"
      "v_mfma_f32_16x16x32_bf16 v[218:221], v[198:201], v[86:89], v[218:221]\n\t"
      "buffer_load_dwordx2 v[44:45], %[xj], %[rA], 0 offen offset:0 sc1\n\t"
      "buffer_load_dwordx2 v[46:47], %[xj], %[rA], 0 offen offset:512 sc1\n\t"
      "v_max3_u32 v238, v86, v87, v238\n\t"
      "v_max3_u32 v239, v88, v89, v239\n\t"
      "v_mfma_f32_16x16x32_bf16 v[222:225], v[202:205], v[86:89], v[222:225]\n\t"
      "v_add_u32 %[wc], %[d2], %[wc]\n\t"
      "v_add_u32 %[wd], %[d2], %[wd]\n\t"
      "ds_read_b64_tr_b16 v[178:179], %[wc] offset:0\n\t"
      "ds_read_b64_tr_b16 v[180:181], %[wc] offset:640\n\t"
      "v_mfma_f32_16x16x32_bf16 v[226:229], v[206:209], v[86:89], v[226:229]\n\t"
      "ds_read_b64_tr_b16 v[182:183], %[wc] offset:32\n\t"
      "ds_read_b64_tr_b16 v[184:185], %[wc] offset:672\n\t"
      "ds_read_b64_tr_b16 v[186:187], %[wc] offset:64\n\t"
      "ds_read_b64_tr_b16 v[188:189], %[wc] offset:704\n\t"
      "v_mfma_f32_16x16x32_bf16 v[230:233], v[210:213], v[86:89], v[230:233]\n\t"
      "ds_read_b64_tr_b16 v[190:191], %[wc] offset:96\n\t"
      "ds_read_b64_tr_b16 v[192:193], %[wc] offset:736\n\t"
      "ds_read_b64_tr_b16 v[194:195], %[wc] offset:128\n\t"
      "ds_read_b64_tr_b16 v[196:197], %[wc] offset:768\n\t"
      "v_mfma_f32_16x16x32_bf16 v[234:237], v[214:217], v[86:89], v[234:237]\n\t"
      "s_waitcnt vmcnt(19) lgkmcnt(15)\n\t"
      "v_mfma_f32_16x16x32_bf16 v[218:221], v[138:141], v[90:93], v[218:221]\n\t"
      "buffer_load_dwordx2 v[48:49], %[xj], %[rA], 0 offen offset:1024 sc1\n\t"
      "v_max3_u32 v238, v90, v91, v238\n\t"
      "v_max3_u32 v239, v92, v93, v239\n\t"
      "v_mfma_f32_16x16x32_bf16 v[222:225], v[142:145], v[90:93], v[222:225]\n\t"
      "ds_read_b64_tr_b16 v[198:199], %[wc] offset:1280\n\t"
      "ds_read_b64_tr_b16 v[200:201], %[wc] offset:1920\n\t"
      "ds_read_b64_tr_b16 v[202:203], %[wc] offset:1312\n\t"
      "v_mfma_f32_16x16x32_bf16 v[226:229], v[146:149], v[90:93], v[226:229]\n\t"
      "ds_read_b64_tr_b16 v[204:205], %[wc] offset:1952\n\t"
      "ds_read_b64_tr_b16 v[206:207], %[wc] offset:1344\n\t"
      "ds_read_b64_tr_b16 v[208:209], %[wc] offset:1984\n\t"
      "v_mfma_f32_16x16x32_bf16 v[230:233], v[150:153], v[90:93], v[230:233]\n\t"
      "ds_read_b64_tr_b16 v[210:211], %[wc] offset:1376\n\t"
      "ds_read_b64_tr_b16 v[212:213], %[wc] offset:2016\n\t"
      "ds_read_b64_tr_b16 v[214:215], %[wc] offset:1408\n\t"
      "v_mfma_f32_16x16x32_bf16 v[234:237], v[154:157], v[90:93], v[234:237]\n\t"
      "ds_read_b64_tr_b16 v[216:217], %[wc] offset:2048\n\t"
      "s_waitcnt vmcnt(19) lgkmcnt(15)\n\t"
      "v_mfma_f32_16x16x32_bf16 v[218:221], v[158:161], v[94:97], v[218:221]\n\t"
      "v_max3_u32 v238, v94, v95, v238\n\t"
      "v_max3_u32 v239, v96, v97, v239\n\t"
      "ds_read_b64_tr_b16 v[138:139], %[wc] offset:12800\n\t"
      "v_mfma_f32_16x16x32_bf16 v[222:225], v[162:165], v[94:97], v[222:225]\n\t"
      "ds_read_b64_tr_b16 v[140:141], %[wc] offset:13440\n\t"
      "ds_read_b64_tr_b16 v[142:143], %[wc] offset:12832\n\t"
      "ds_read_b64_tr_b16 v[144:145], %[wc] offset:13472\n\t"
      "v_mfma_f32_16x16x32_bf16 v[226:229], v[166:169], v[94:97], v[226:229]\n\t"
      "ds_read_b64_tr_b16 v[146:147], %[wc] offset:12864\n\t"
      "ds_read_b64_tr_b16 v[148:149], %[wc] offset:13504\n\t"
      "ds_read_b64_tr_b16 v[150:151], %[wc] offset:12896\n\t"
      "v_mfma_f32_16x16x32_bf16 v[230:233], v[170:173], v[94:97], v[230:233]\n\t"
      "ds_read_b64_tr_b16 v[152:153], %[wc] offset:13536\n\t"
      "ds_read_b64_tr_b16 v[154:155], %[wc] offset:12928\n\t"
      "ds_read_b64_tr_b16 v[156:157], %[wc] offset:13568\n\t"
      "v_mfma_f32_16x16x32_bf16 v[234:237], v[174:177], v[94:97], v[234:237]\n\t"
      "s_waitcnt vmcnt(18) lgkmcnt(15)\n\t"
      "v_mfma_f32_16x16x32_bf16 v[218:221], v[178:181], v[98:101], v[218:221]\n\t"
      "v_max3_u32 v238, v98, v99, v238\n\t"
      "v_max3_u32 v239, v100, v101, v239\n\t"
      "ds_read_b64_tr_b16 v[158:159], %[wc] offset:14080\n\t"
      "v_mfma_f32_16x16x32_bf16 v[222:225], v[182:185], v[98:101], v[222:225]\n\t"
      "ds_read_b64_tr_b16 v[160:161], %[wc] offset:14720\n\t"
      "ds_read_b64_tr_b16 v[162:163], %[wc] offset:14112\n\t"
      "ds_read_b64_tr_b16 v[164:165], %[wc] offset:14752\n\t"
      "v_mfma_f32_16x16x32_bf16 v[226:229], v[186:189], v[98:101], v[226:229]\n\t"
      "ds_read_b64_tr_b16 v[166:167], %[wc] offset:14144\n\t"
      "ds_read_b64_tr_b16 v[168:169], %[wc] offset:14784\n\t"
      "ds_read_b64_tr_b16 v[170:171], %[wc] offset:14176\n\t"
      "v_mfma_f32_16x16x32_bf16 v[230:233], v[190:193], v[98:101], v[230:233]\n\t"
      "ds_read_b64_tr_b16 v[172:173], %[wc] offset:14816\n\t"
      "ds_read_b64_tr_b16 v[174:175], %[wc] offset:14208\n\t"
      "ds_read_b64_tr_b16 v[176:177], %[wc] offset:14848\n\t"
      "v_mfma_f32_16x16x32_bf16 v[234:237], v[194:197], v[98:101], v[234:237]\n\t"
      "s_waitcnt vmcnt(17) lgkmcnt(15)\n\t"
      "v_mfma_f32_16x16x32_bf16 v[218:221], v[198:201], v[102:105], v[218:221]\n\t"
      "v_max3_u32 v238, v102, v103, v238\n\t"
      "v_max3_u32 v239, v104, v105, v239\n\t"
      "ds_read_b64_tr_b16 v[178:179], %[wd] offset:2560\n\t"
      "v_mfma_f32_16x16x32_bf16 v[222:225], v[202:205], v[102:105], v[222:225]\n\t"
      "ds_read_b64_tr_b16 v[180:181], %[wd] offset:5760\n\t"
      "ds_read_b64_tr_b16 v[182:183], %[wd] offset:2592\n\t"
      "ds_read_b64_tr_b16 v[184:185], %[wd] offset:5792\n\t"
      "v_mfma_f32_16x16x32_bf16 v[226:229], v[206:209], v[102:105], v[226:229]\n\t"
      "ds_read_b64_tr_b16 v[186:187], %[wd] offset:2624\n\t"
      "ds_read_b64_tr_b16 v[188:189], %[wd] offset:5824\n\t"
      "ds_read_b64_tr_b16 v[190:191], %[wd] offset:2656\n\t"
      "v_mfma_f32_16x16x32_bf16 v[230:233], v[210:213], v[102:105], v[230:233]\n\t"
      "ds_read_b64_tr_b16 v[192:193], %[wd] offset:5856\n\t"
      "ds_read_b64_tr_b16 v[194:195], %[wd] offset:2688\n\t"
      "ds_read_b64_tr_b16 v[196:197], %[wd] offset:5888\n\t"
      "v_mfma_f32_16x16x32_bf16 v[234:237], v[214:217], v[102:105], v[234:237]\n\t"
      "s_waitcnt vmcnt(16) lgkmcnt(15)\n\t"
      "v_mfma_f32_16x16x32_bf16 v[218:221], v[138:141], v[106:109], v[218:221]\n\t"
      "v_max3_u32 v238, v106, v107, v238\n\t"
      "v_max3_u32 v239, v108, v109, v239\n\t"
      "v_add_u32 %[wc], %[d3], %[wc]\n\t"
      "v_mfma_f32_16x16x32_bf16 v[222:225], v[142:145], v[106:109], v[222:225]\n\t"
      "v_add_u32 %[wd], %[d3], %[wd]\n\t"
      "ds_read_b64_tr_b16 v[198:199], %[wc] offset:0\n\t"
      "ds_read_b64_tr_b16 v[200:201], %[wc] offset:640\n\t"
      "v_mfma_f32_16x16x32_bf16 v[226:229], v[146:149], v[106:109], v[226:229]\n\t"
      "ds_read_b64_tr_b16 v[202:203], %[wc] offset:32\n\t"
      "ds_read_b64_tr_b16 v[204:205], %[wc] offset:672\n\t"
      "ds_read_b64_tr_b16 v[206:207], %[wc] offset:64\n\t"
      "v_mfma_f32_16x16x32_bf16 v[230:233], v[150:153], v[106:109], v[230:233]\n\t"
      "ds_read_b64_tr_b16 v[208:209], %[wc] offset:704\n\t"
      "ds_read_b64_tr_b16 v[210:211], %[wc] offset:96\n\t"
      "ds_read_b64_tr_b16 v[212:213], %[wc] offset:736\n\t"
      "v_mfma_f32_16x16x32_bf16 v[234:237], v[154:157], v[106:109], v[234:237]\n\t"
      "ds_read_b64_tr_b16 v[214:215], %[wc] offset:128\n\t"
      "ds_read_b64_tr_b16 v[216:217], %[wc] offset:768\n\t"
      "s_waitcnt vmcnt(15) lgkmcnt(15)\n\t"
      "v_mfma_f32_16x16x32_bf16 v[218:221], v[158:161], v[110:113], v[218:221]\n\t"
      "v_max3_u32 v238, v110, v111, v238\n\t"
      "v_max3_u32 v239, v112, v113, v239\n\t"
      "ds_read_b64_tr_b16 v[138:139], %[wc] offset:1280\n\t"
      "v_mfma_f32_16x16x32_bf16 v[222:225], v[162:165], v[110:113], v[222:225]\n\t"
      "ds_read_b64_tr_b16 v[140:141], %[wc] offset:1920\n\t"
      "ds_read_b64_tr_b16 v[142:143], %[wc] offset:1312\n\t"
      "ds_read_b64_tr_b16 v[144:145], %[wc] offset:1952\n\t"
      "v_mfma_f32_16x16x32_bf16 v[226:229], v[166:169], v[110:113], v[226:229]\n\t"
      "ds_read_b64_tr_b16 v[146:147], %[wc] offset:1344\n\t"
      "ds_read_b64_tr_b16 v[148:149], %[wc] offset:1984\n\t"
      "ds_read_b64_tr_b16 v[150:151], %[wc] offset:1376\n\t"
      "v_mfma_f32_16x16x32_bf16 v[230:233], v[170:173], v[110:113], v[230:233]\n\t"
      "ds_read_b64_tr_b16 v[152:153], %[wc] offset:2016\n\t"
      "ds_read_b64_tr_b16 v[154:155], %[wc] offset:1408\n\t"
      "ds_read_b64_tr_b16 v[156:157], %[wc] offset:2048\n\t"
      "v_mfma_f32_16x16x32_bf16 v[234:237], v[174:177], v[110:113], v[234:237]\n\t"
      "s_waitcnt vmcnt(14) lgkmcnt(15)\n\t"
      "v_mfma_f32_16x16x32_bf16 v[218:221], v[178:181], v[114:117], v[218:221]\n\t"
      "v_max3_u32 v238, v114, v115, v238\n\t"
      "v_max3_u32 v239, v116, v117, v239\n\t"
      "ds_read_b64_tr_b16 v[158:159], %[wc] offset:12800\n\t"
      "v_mfma_f32_16x16x32_bf16 v[222:225], v[182:185], v[114:117], v[222:225]\n\t"
      "ds_read_b64_tr_b16 v[160:161], %[wc] offset:13440\n\t"
      "ds_read_b64_tr_b16 v[162:163], %[wc] offset:12832\n\t"
      "ds_read_b64_tr_b16 v[164:165], %[wc] offset:13472\n\t"
      "v_mfma_f32_16x16x32_bf16 v[226:229], v[186:189], v[114:117], v[226:229]\n\t"
      "ds_read_b64_tr_b16 v[166:167], %[wc] offset:12864\n\t"
      "ds_read_b64_tr_b16 v[168:169], %[wc] offset:13504\n\t"
      "ds_read_b64_tr_b16 v[170:171], %[wc] offset:12896\n\t"
      "v_mfma_f32_16x16x32_bf16 v[230:233], v[190:193], v[114:117], v[230:233]\n\t"
      "ds_read_b64_tr_b16 v[172:173], %[wc] offset:13536\n\t"
      "ds_read_b64_tr_b16 v[174:175], %[wc] offset:12928\n\t"
      "ds_read_b64_tr_b16 v[176:177], %[wc] offset:13568\n\t"
      "v_mfma_f32_16x16x32_bf16 v[234:237], v[194:197], v[114:117], v[234:237]\n\t"
      "s_waitcnt vmcnt(13) lgkmcnt(15)\n\t"
      "v_mfma_f32_16x16x32_bf16 v[218:221], v[198:201], v[118:121], v[218:221]\n\t"
      "v_max3_u32 v238, v118, v119, v238\n\t"
      "v_max3_u32 v239, v120, v121, v239\n\t"
      "ds_read_b64_tr_b16 v[178:179], %[wc] offset:14080\n\t"
      "v_mfma_f32_16x16x32_bf16 v[222:225], v[202:205], v[118:121], v[222:225]\n\t"
      "ds_read_b64_tr_b16 v[180:181], %[wc] offset:14720\n\t"
      "ds_read_b64_tr_b16 v[182:183], %[wc] offset:14112\n\t"
      "ds_read_b64_tr_b16 v[184:185], %[wc] offset:14752\n\t"
      "v_mfma_f32_16x16x32_bf16 v[226:229], v[206:209], v[118:121], v[226:229]\n\t"
      "ds_read_b64_tr_b16 v[186:187], %[wc] offset:14144\n\t"
      "ds_read_b64_tr_b16 v[188:189], %[wc] offset:14784\n\t"
      "ds_read_b64_tr_b16 v[190:191], %[wc] offset:14176\n\t"
      "v_mfma_f32_16x16x32_bf16 v[230:233], v[210:213], v[118:121], v[230:233]\n\t"
      "ds_read_b64_tr_b16 v[192:193], %[wc] offset:14816\n\t"
      "ds_read_b64_tr_b16 v[194:195], %[wc] offset:14208\n\t"
      "ds_read_b64_tr_b16 v[196:197], %[wc] offset:14848\n\t"
      "v_mfma_f32_16x16x32_bf16 v[234:237], v[214:217], v[118:121], v[234:237]\n\t"
      "s_waitcnt vmcnt(12) lgkmcnt(15)\n\t"
      "v_mfma_f32_16x16x32_bf16 v[218:221], v[138:141], v[122:125], v[218:221]\n\t"
      "v_max3_u32 v238, v122, v123, v238\n\t"
      "v_max3_u32 v239, v124, v125, v239\n\t"
      "ds_read_b64_tr_b16 v[198:199], %[wd] offset:2560\n\t"
      "v_mfma_f32_16x16x32_bf16 v[222:225], v[142:145], v[122:125], v[222:225]\n\t"
      "ds_read_b64_tr_b16 v[200:201], %[wd] offset:5760\n\t"
      "ds_read_b64_tr_b16 v[202:203], %[wd] offset:2592\n\t"
      "ds_read_b64_tr_b16 v[204:205], %[wd] offset:5792\n\t"
      "v_mfma_f32_16x16x32_bf16 v[226:229], v[146:149], v[122:125], v[226:229]\n\t"
      "ds_read_b64_tr_b16 v[206:207], %[wd] offset:2624\n\t"
      "ds_read_b64_tr_b16 v[208:209], %[wd] offset:5824\n\t"
      "ds_read_b64_tr_b16 v[210:211], %[wd] offset:2656\n\t"
      "v_mfma_f32_16x16x32_bf16 v[230:233], v[150:153], v[122:125], v[230:233]\n\t"
      "ds_read_b64_tr_b16 v[212:213], %[wd] offset:5856\n\t"
      "ds_read_b64_tr_b16 v[214:215], %[wd] offset:2688\n\t"
      "ds_read_b64_tr_b16 v[216:217], %[wd] offset:5888\n\t"
      "v_mfma_f32_16x16x32_bf16 v[234:237], v[154:157], v[122:125], v[234:237]\n\t"
      "s_waitcnt vmcnt(11) lgkmcnt(15)\n\t"
      "v_mfma_f32_16x16x32_bf16 v[218:221], v[158:161], v[126:129], v[218:221]\n\t"
      "v_max3_u32 v238, v126, v127, v238\n\t"
      "v_mfma_f32_16x16x32_bf16 v[222:225], v[162:165], v[126:129], v[222:225]\n\t"
      "v_max3_u32 v239, v128, v129, v239\n\t"
      "v_mfma_f32_16x16x32_bf16 v[226:229], v[166:169], v[126:129], v[226:229]\n\t"
      "v_mfma_f32_16x16x32_bf16 v[230:233], v[170:173], v[126:129], v[230:233]\n\t"
      "v_mfma_f32_16x16x32_bf16 v[234:237], v[174:177], v[126:129], v[234:237]\n\t"
      "s_waitcnt vmcnt(10) lgkmcnt(10)\n\t"
      "v_mfma_f32_16x16x32_bf16 v[218:221], v[178:181], v[130:133], v[218:221]\n\t"
      "v_max3_u32 v238, v130, v131, v238\n\t"
      "v_mfma_f32_16x16x32_bf16 v[222:225], v[182:185], v[130:133], v[222:225]\n\t"
      "v_max3_u32 v239, v132, v133, v239\n\t"
      "v_mfma_f32_16x16x32_bf16 v[226:229], v[186:189], v[130:133], v[226:229]\n\t"
      "v_mfma_f32_16x16x32_bf16 v[230:233], v[190:193], v[130:133], v[230:233]\n\t"
      "v_mfma_f32_16x16x32_bf16 v[234:237], v[194:197], v[130:133], v[234:237]\n\t"
      "s_waitcnt vmcnt(9) lgkmcnt(0)\n\t"
      "v_mfma_f32_16x16x32_bf16 v[218:221], v[198:201], v[134:137], v[218:221]\n\t"
      "v_max3_u32 v238, v134, v135, v238\n\t"
      "v_mfma_f32_16x16x32_bf16 v[222:225], v[202:205], v[134:137], v[222:225]\n\t"
      "v_max3_u32 v239, v136, v137, v239\n\t"
      "v_mfma_f32_16x16x32_bf16 v[226:229], v[206:209], v[134:137], v[226:229]\n\t"
      "v_mfma_f32_16x16x32_bf16 v[230:233], v[210:213], v[134:137], v[230:233]\n\t"
      "v_mfma_f32_16x16x32_bf16 v[234:237], v[214:217], v[134:137], v[234:237]\n\t"
      AST(4)
      "v_max_u32 v238, v238, v239\n\t"
      "v_cmp_eq_u32 vcc, -1, v238\n\t"
      "s_cbranch_vccz 2f\n\t"
      "v_add_u32 %[wc], %[d0], %[wc]\n\t"
      "v_add_u32 %[wd], %[d0], %[wd]\n\t"
      "s_sub_u32 %[cnt], %[cnt], 0x100\n\t"
      "s_cbranch_scc0 7b\n\t"
      "2:\n\t"
      "s_cmp_eq_u32 %[late], 0\n\t"
      "s_waitcnt vmcnt(0)\n\t"
      "s_cbranch_scc1 9f\n\t"
      "s_mov_b32 %[cnt], 0x40000\n\t"
      "3:\n\t"
      "buffer_load_dwordx2 v[130:131], %[fa], %[rA], 0 offen sc1\n\t"
      "s_sub_u32 %[cnt], %[cnt], 1\n\t"
      "s_cbranch_scc1 4f\n\t"
      "s_waitcnt vmcnt(0)\n\t"
      "v_cmp_eq_u64 vcc, %[ex], v[130:131]\n\t"
      "s_andn2_b32 vcc_hi, %[late], vcc_hi\n\t"
      "s_cbranch_scc1 3b\n\t"
      "4:\n\t"
      "s_movk_i32 %[late], 0x1000\n\t"
      "buffer_load_dwordx4 v[20:23], %[xi], %[rA], 0 offen offset:0 sc1\n\t"
      "buffer_load_dwordx4 v[24:27], %[xi], %[rA], 0 offen offset:1024 sc1\n\t"
      "buffer_load_dwordx4 v[28:31], %[xi], %[rA], 0 offen offset:2048 sc1\n\t"
      "buffer_load_dwordx4 v[32:35], %[xi], %[rA], 0 offen offset:3072 sc1\n\t"
      "buffer_load_dwordx4 v[36:39], %[xi], %[rA], %[late] offen offset:0 sc1\n\t"
      "buffer_load_dwordx4 v[40:43], %[xi], %[rA], %[late] offen offset:1024 sc1\n\t"
      "buffer_load_dwordx2 v[44:45], %[xj], %[rA], 0 offen offset:0 sc1\n\t"
      "buffer_load_dwordx2 v[46:47], %[xj], %[rA], 0 offen offset:512 sc1\n\t"
      "buffer_load_dwordx2 v[48:49], %[xj], %[rA], 0 offen offset:1024 sc1\n\t"
      "s_mov_b32 %[late], 0\n\t"
      "s_waitcnt vmcnt(0)\n\t"
      "9:\n\t"
      "s_nop 7\n\t"
      "v_mfma_f32_16x16x32_bf16 v[0:3], v[50:53], v[20:23], v[218:221]\n\t"
      "v_mfma_f32_16x16x32_bf16 v[4:7], v[54:57], v[20:23], v[222:225]\n\t"
      "v_mfma_f32_16x16x32_bf16 v[8:11], v[50:53], v[24:27], v[226:229]\n\t"
      "v_mfma_f32_16x16x32_bf16 v[12:15], v[54:57], v[24:27], v[230:233]\n\t"
      "v_mfma_f32_16x16x32_bf16 v[16:19], v[50:53], v[44:47], v[234:237]\n\t"
      "v_mfma_f32_16x16x32_bf16 v[0:3], v[50:53], v[28:31], v[0:3]\n\t"
      "v_mfma_f32_16x16x32_bf16 v[4:7], v[54:57], v[28:31], v[4:7]\n\t"
      "v_mfma_f32_16x16x32_bf16 v[8:11], v[50:53], v[32:35], v[8:11]\n\t"
      "v_mfma_f32_16x16x32_bf16 v[12:15], v[54:57], v[32:35], v[12:15]\n\t"
      "v_mfma_f32_16x16x32_bf16 v[16:19], v[54:57], v[44:47], v[16:19]\n\t"
      "v_mfma_f32_16x16x32_bf16 v[0:3], v[50:53], v[36:39], v[0:3]\n\t"
      "v_mfma_f32_16x16x32_bf16 v[4:7], v[54:57], v[36:39], v[4:7]\n\t"
      "v_mfma_f32_16x16x32_bf16 v[8:11], v[50:53], v[40:43], v[8:11]\n\t"
      "v_mfma_f32_16x16x32_bf16 v[12:15], v[54:57], v[40:43], v[12:15]\n\t"
      "v_mfma_f32_16x16x32_bf16 v[16:19], v[54:57], v[46:49], v[16:19]\n\t"
      "s_nop 7\n\t"
      "s_nop 1\n\t"
        : "+{v[0:15]}"(s0), "+{v[16:19]}"(s1), [so] "+&s"(so), [wc] "+&v"(wc), [wd] "+&v"(wd), [cnt] "=&s"(cnt), [c2] "=&s"(c2), [late] "=&s"(late) AST_OUT
        : "{v[50:53]}"(e0), "{v[54:57]}"(e1), [fa] "v"(fo), [ex] "v"(ex), [va] "v"(va), [pw] "v"(pw), [xi] "v"(xi), [xj] "v"(xj), [rA] "s"(rWS),
          [sb] "s"(sb), [se] "s"(sb + 81920), [d0] "s"(dl[0]), [d1] "s"(dl[1]), [d2] "s"(dl[2]), [d3] "s"(dl[3])
        : "memory", "scc", "vcc", "v20", "v21", "v22", "v23", "v24", "v25", "v26", "v27", "v28", "v29", "v30", "v31", "v32", "v33", "v34", "v35", "v36", "v37", "v38", "v39", "v40", "v41", "v42", "v43", "v44", "v45", "v46", "v47", "v48", "v49", "v58", "v59", "v60", "v61", "v62", "v63", "v64", "v65", "v66", "v67", "v68", "v69", "v70", "v71", "v72", "v73", "v74", "v75", "v76", "v77", "v78", "v79", "v80", "v81", "v82", "v83", "v84", "v85", "v86", "v87", "v88", "v89", "v90", "v91", "v92", "v93", "v94", "v95", "v96", "v97", "v98", "v99", "v100", "v101", "v102", "v103", "v104", "v105", "v106", "v107", "v108", "v109", "v110", "v111", "v112", "v113", "v114", "v115", "v116", "v117", "v118", "v119", "v120", "v121", "v122", "v123", "v124", "v125", "v126", "v127", "v128", "v129", "v130", "v131", "v132", "v133", "v134", "v135", "v136", "v137", "v138", "v139", "v140", "v141", "v142", "v143", "v144", "v145", "v146", "v147", "v148", "v149", "v150", "v151", "v152", "v153", "v154", "v155", "v156", "v157", "v158", "v159", "v160", "v161", "v162", "v163", "v164", "v165", "v166", "v167", "v168", "v169", "v170", "v171", "v172", "v173", "v174", "v175", "v176", "v177", "v178", "v179", "v180", "v181", "v182", "v183", "v184", "v185", "v186", "v187", "v188", "v189", "v190", "v191", "v192", "v193", "v194", "v195", "v196", "v197", "v198", "v199", "v200", "v201", "v202", "v203", "v204", "v205", "v206", "v207", "v208", "v209", "v210", "v211", "v212", "v213", "v214", "v215", "v216", "v217", "v218", "v219", "v220", "v221", "v222", "v223", "v224", "v225", "v226", "v227", "v228", "v229", "v230", "v231", "v232", "v233", "v234", "v235", "v236", "v237", "v238", "v239");
      STAMP(9); AST_PUT(25);
    }
    u32 o[10];
#pragma unroll
    for (int i = 0; i < 10; ++i) o[i] = i < 8 ? pk2(s0[2 * i], s0[2 * i + 1]) : pk2(s1[2 * i - 16], s1[2 * i - 15]);
    const rsrc_t rO = mkrsrc(out, S_ * D_ * 2);
#pragma unroll
    for (int p = 0; p < 2; ++p) { const u32x2 u = sw16(o[4 * p], o[4 * p + 2]), v = sw16(o[4 * p + 1], o[4 * p + 3]);
      __builtin_amdgcn_raw_buffer_store_b128((u32x4){u.x, v.x, u.y, v.y}, rO, o1 + 64 * p, 0, OAUX); }
    __builtin_amdgcn_raw_buffer_store_b64((u32x2){o[8], o[9]}, rO, o2, 0, OAUX);
    if (bid == 0 && wave == 0) put_flag(rWS, WS_EPOCH, tag);
    STAMP(10); STAMP_END;
}

__global__ __launch_bounds__(512, 2) void fused(const u16* __restrict__ x, const u16* __restrict__ wqkv, const u16* __restrict__ w1,
                                                char* __restrict__ ws, const u16* __restrict__ wphi, const u16* __restrict__ wo,
                                                const u16* __restrict__ b1, const u16* __restrict__ w2, const u16* __restrict__ b2,
                                                u16* __restrict__ out) {
  STAMP(0);
  u64 pc0; asm volatile("s_getpc_b64 %0" : "=s"(pc0));
  const int wave = rfl(threadIdx.x >> 6);
  const int bid = blockIdx.x, xcd = bid & 7, idx = bid >> 3;
  const int mt = (xcd & 1) * 8 + (idx >> 2), hq = (xcd >> 1) * 2 + (idx & 1), isQ = (idx >> 1) & 1;
  const int kq = xcd >> 1, n4 = idx & 3, kh = kq & 1, n2 = 2 * n4 + (kq >> 1);
  asm volatile("" :: "s"(x), "s"(wqkv), "s"(w1), "s"(ws), "s"(wphi), "s"(wo), "s"(b1), "s"(w2), "s"(b2), "s"(out));
  auto touch8k = [&](u64 p, u32 st = 128) { asm volatile("s_mov_b32 m0, %0\n\ts_nop 0\n\tglobal_load_lds_dword %1, %2" :: "s"(L_KVT + wave * 256), "v"(lane_id() * st), "s"(p) : "memory"); };
  if (CPF && wave == 3 && idx < (CODE_BYTES + 8191) / 8192) touch8k((pc0 & ~255ull) + min((u32)idx * 8192u, CODE_BYTES - 8068u));
  if (TPF && wave == 3 && idx == 6) { const int l = lane_id();
    const u64 pa = l < 48 ? (u64)ws + l * 0x80000 : l < 53 ? (u64)out + (l - 48) * 0x80000 : l < 58 ? (u64)w2 + (l - 53) * 0x80000 : (u64)wo + (l & 1) * 0x80000;
    asm volatile("s_mov_b32 m0, %0\n\ts_nop 0\n\tglobal_load_lds_dword %1, off" :: "s"(L_KVT + 768), "v"(pa) : "memory"); }
  if (SPF && wave == 2 && bid < 32) touch8k((u64)ws + (bid < 16 ? WS_CSUM : WS_STATS - 0x20000) + bid * 8192);
  if (WOT && wave == 1 && bid < 100) touch8k((u64)wo + bid * 8192);
  if (wave < NE) touch8k((wave ? (u64)w1 : (u64)wqkv) + bid * 8192);
  const rsrc_t rWS = mkrsrc(ws, WS_END), rX = mkrsrc(x, S_ * D_ * 2);
  u64 tag = *(const __attribute__((address_space(4))) u64*)ws;
  auto mktag = [&]() { asm volatile("" : "+s"(tag)); tag = tag * 0x9E3779B97F4A7C15ull + 0x632BE59BD9B4E019ull; asm volatile("" : "+s"(tag)); };

  if (__builtin_expect(wave >= 4, 1)) {
    const int a = wave - 4;
    const int lane = lane_id(), g = lane >> 4, c16 = lane & 15, q4 = c16 >> 2, p4 = lane & 3;
    const u32 trow = 4 * g + q4;
    const int rot = (xcd ^ (xcd >> 1)) & 1;
    const bool lo = a < 2, uw1 = lo == (bool)isQ;
    const u32 xp = lo ? 2 * a : 6 * a - 8;
    const u32 vx0 = (lane >> 3) * 1280 + (((lane & 7) ^ g) * 16), vx1 = (vx0 ^ 64) + 10240;
    u32 kk = 5 * rot + ((idx >> 2) + (idx & 3) + (xcd & 4)) % 5, sb = SL0, kend = 5 * rot + 4;
#define M0A(d) "s_add_u32 m0, m0, " #d "\n\t"
#define NOP "s_nop 0\n\t"
#define LD(r, o) "buffer_load_dwordx4 %[v" #r "], %[rs], %[" #o "] offen lds\n\t"
    auto issx = [&]() { u32 t;
      asm volatile("s_mov_b32 m0, %[m]\n\t" NOP LD(0, s) M0A(1024) "s_cmp_lt_u32 %[a], 2\n\t" LD(1, s) "s_cbranch_scc1 1f\n\t"
                   M0A(1024) "s_add_u32 %[t], %[s], 20480\n\t" LD(0, t) M0A(1024) NOP LD(1, t)
                   M0A(1024) "s_add_u32 %[t], %[t], 20480\n\t" LD(0, t) M0A(1024) NOP LD(1, t) "1:"
                   : [t] "=&s"(t) : [v0] "v"(vx0), [v1] "v"(vx1), [rs] "s"(rX), [s] "s"((mt * 16 + xp) * 10240 + kk * 128), [m] "s"(sb + xp * 1024), [a] "s"(a) : "memory", "scc"); };
    STAMP(1);
    issx();
    const u32 wst = uw1 ? 3456 : 3840;
    const u32 sw0 = (isQ ? 0 : lo ? 1280 : 2560) + (uw1 ? (lo ? 320 : 128) : 160) * hq + (a & 1) * 32 * wst;
    const rsrc_t rW = mkrsrc(uw1 ? w1 : wqkv, 640 * wst);
    const u32 wl = lo ? W_OFF + a * 10240 : C_OFF - 10240 + a * 5120;
    const u32 dB = isQ ? 0 : lo ? 1120 : -128, lB = lo ? 160 : 128, sh = lo ? 12 : 11, cpr = lo ? 320 : 160, xm = lo ? 32 : 0;
    u32 vw[5];
#pragma unroll
    for (int i = 0; i < 5; ++i) { const u32 c = 64 * i + lane, r = (c * 205) >> sh, o = (c * 16 - cpr * r) ^ ((r * 8) & xm);
      vw[i] = ((r & 3) + 2 * (r & 12) + (r >> 4) * 4) * wst + o + (o >= lB ? dB : 0); }
#define W5(o) LD(0, o) M0A(1024) NOP LD(1, o) M0A(1024) NOP LD(2, o) M0A(1024) NOP LD(3, o) M0A(1024) "s_cmp_lt_u32 %[a], 2\n\t" LD(4, o)
    auto issw = [&]() { u32 t;
      asm volatile("s_mov_b32 m0, %[m]\n\ts_add_u32 %[t], %[s], %[d]\n\t" W5(s) "s_cbranch_scc0 1f\n\t" M0A(1024) NOP W5(t) "1:"
                   : [t] "=&s"(t) : [v0] "v"(vw[0]), [v1] "v"(vw[1]), [v2] "v"(vw[2]), [v3] "v"(vw[3]), [v4] "v"(vw[4]), [rs] "s"(rW),
                     [s] "s"(sw0 + kk * 64 * wst), [d] "s"(4 * wst), [m] "s"(sb + wl), [a] "s"(a) : "memory", "scc");
      kk = KW ? (kk == kend ? kk - 4 : kk + 1) : (kk == 9 ? 0 : kk + 1); sb = sb == SL0 ? SL1 : sb + SLOT; };
    issw();
    STAMP(2);
    mktag();
    const u32 tag32 = (u32)tag, csb = WS_CSUM + (isQ * 8 + hq) * 4096;
    u32 vA[5], vB[5], fa[6], so2[7];
#pragma unroll
    for (int i = 0; i < 5; ++i) { const u32 c = 64 * i + lane, r = (c * 205) >> 12, o = (c * 16 - 320 * r) ^ ((r * 8) & 32);
      vA[i] = r * 1280 + o; vB[i] = (r + (r & 12)) * 1280 + o; pin(vA[i]); pin(vB[i]); }
#pragma unroll
    for (int i = 0; i < 6; ++i) { const u32 d = 32 * (i >> 1) + 8 * g + 4 * (i & 1) + q4; fa[i] = L_RAW + (d - 20 * p4 < 20 ? 40 * d : 3200); pin(fa[i]); }
#pragma unroll
    for (int k = 0; k < 7; ++k) {
      const int n = 2 * k + (a >> 1), hi = n >= 7, ph = 2 * kq + hi, pr = n - 7 * hi;
      so2[k] = ((pr < 5 ? 160 : 64) * ph + (pr < 4 ? 32 * pr + 16 * (pr & 2) : pr == 4 ? 64 : 1120 + 32 * pr) + (a & 1) * (pr == 4 ? 4 : 16)) * 1280 + n4 * 320;
      asm volatile("" : "+s"(so2[k])); }
    const bool cson = 4 * a + g < 15;
    u32 gco = cson ? csb + (64 * a + lane) * 16 : OOB, gso = WS_STATS + (mt * 256 + 64 * a + lane) * 16;
    u32 brow = (isQ ? L_QI + (32 * a + c16) * 160 : L_KI + (32 * a + c16) * 480); const u32 bst = isQ ? 2560 : 7680;
    u64 tp = a == 3 ? (u64)w2 + bid * 8192 : a != 2 || bid >= 114 ? (u64)w2 : bid < 14 ? (u64)w2 + (256 + bid) * 8192 : (u64)wo + (bid - 14) * 8192;
    u32 tv = lane * (a == 3 || (a == 2 && bid < ((TILE && KT) || WOT ? 14 : 114)) ? 128 : 0);
    if (XT) {
      tp = (u64)w2 + (a >= 2 ? kq * 409600 + idx * 12800 + (a & 1) * 6400 : a == 1 && (kq < 3 || idx < 16) ? 1638400 + kq * 163840 + idx * 5120 : 0);
      tv = min(lane, a >= 2 ? 49 : a == 1 && (kq < 3 || idx < 16) ? 39 : 0) * 128; }
    pin(gco); pin(gso); pin(brow); pin(tv); asm volatile("" : "+s"(tp));
    issx();
    const int ju = (mt & 7) * 4 + a, jks = ju >= 15, jtu = ju - 15 * jks, w8 = (xcd >> 2) * 4 + (idx & 3);
    u32 jw, jv, jx;
    { const int c = jtu >= 7, t = jtu - 7 * c;
      const u32 o = t == 2 ? 128 + 32 * c : t >= 7 ? 128 : 64 * c + 32 * t + (t < 2 ? 0 : t < 5 ? 96 : -160), rs = t < 5 ? 320 : 160;
      jw = (t < 5 ? W_OFF : C_OFF) + (32 * jks + trow) * rs + 8 * p4 + (o ^ ((lane * 2) & (t < 5 ? 32 : 0))); jv = jw + 16 * rs;
      jx = (16 * w8 + c16) * 128 + ((((a & 1) * 4 + g) ^ (c16 >> 1)) * 16); }
    u32 jo = q4 == g ? WS_STATS + rot * ST_PLANE + (mt * 128 + 16 * w8 + c16) * 32 + (a >> 1) * 16 + (a & 1) * 8
                     : q4 == (g ^ 1) && ju < 30 ? csb + rot * CS_PLANE + (jtu * 16 + c16) * 16 + jks * 8 : OOB;
    pin(jw); pin(jv); pin(jx); pin(jo);
    u32x4 wc0, wc1; u32 wd0, wd1, ws0, ws1;
    const rsrc_t rWo = mkrsrc(wo, D_ * D_ * 2);
    { const u32 c0 = a * 100 + lane, c1 = c0 + 64;
      ws0 = ((c0 * 205) >> 11) * 1120 + c0 * 16; ws1 = lane < 36 ? ((c1 * 205) >> 11) * 1120 + c1 * 16 : OOB;
      wd0 = WS_WO + (isQ * 8 + hq) * 102400 + mt * 6400 + c0 * 16; wd1 = lane < 36 ? wd0 + 1024 : OOB;
      if (!KT && !isQ) ws0 = ws1 = wd0 = wd1 = OOB; pin(wd0); pin(wd1); pin(ws0); pin(ws1);
      asm volatile("" : "=v"(wc0), "=v"(wc1)); }
    issw();
    if (TILE == 2) asm volatile("buffer_load_dwordx4 %0, %2, %4, %5 offen\n\tbuffer_load_dwordx4 %1, %3, %4, %5 offen"
                               :: "v"(wc0), "v"(wc1), "v"(ws0), "v"(ws1), "s"(rWo), "s"(mt * 51200 + n2 * 160) : "memory");
    STAMP(3);
    const u32x4 ones = {0x3f803f80u, 0x3f803f80u, 0x3f803f80u, 0x3f803f80u};
    f32x4 cacc = {0.f, 0.f, 0.f, 0.f}, sacc = cacc;
    u32 sj = SL0;
    __builtin_amdgcn_s_setprio(3);
    UNROLL_S
    for (int s = 0; s < (L8 ? 8 : 5); ++s) {
      if (TILE == 2) { if (s == 2 || s == 4) wait_vm<11>(); } else if (TILE && s == 4) wait_vm<13>(); else wait_vm<11>();
      wait_bar(13, s);
      bf16x8 wf = tr16(jw + sj, jv + sj); u32x4 xf = lds128(jx + sj); sj = sj == SL0 ? SL1 : sj + SLOT;
      if (TILE && s == (TILE == 2 ? 2 : 3)) { st128h(wc0, rWS, wd0); st128h(wc1, rWS, wd1); }

      issx();
      if (L8 && s == 5) st64((u32x2){__float_as_uint(q4 != g ? cacc[0] : p4 == 0 ? sacc[0] : p4 == 1 ? sacc[1] : p4 == 2 ? sacc[2] : sacc[3]), tag32}, rWS, jo);
      pin(wf); pin(xf); cacc = mfma32(as8(ones), wf, cacc); sacc = mfma32(as8(lo ? ones : xf), as8(xf), sacc); pin(cacc); pin(sacc);
      issw();
      if (KW && s == 2) { kk += 5 - 10 * rot; kend += 5 - 10 * rot; }
      if (TILE == 1 && s == 1) asm volatile("buffer_load_dwordx4 %0, %2, %4, %5 offen\n\tbuffer_load_dwordx4 %1, %3, %4, %5 offen"
                               :: "v"(wc0), "v"(wc1), "v"(ws0), "v"(ws1), "s"(rWo), "s"(mt * 51200 + n2 * 160) : "memory"); }
    if (!L8) {
    wait_bar(11, 5); issx();
    st64((u32x2){__float_as_uint(q4 != g ? cacc[0] : p4 == 0 ? sacc[0] : p4 == 1 ? sacc[1] : p4 == 2 ? sacc[2] : sacc[3]), tag32}, rWS, jo);
    issw();
    wait_bar(12, 6); issx(); issw();
    wait_bar(11, 7); issx(); issw();
    }
    u32x4 gc[2], gs[2];
    auto gather = [&]() { gc[0] = ld128c(rWS, gco, 0); gc[1] = ld128c(rWS, gco, CS_PLANE); gs[0] = ld128c(rWS, gso, 0); gs[1] = ld128c(rWS, gso, ST_PLANE); };
    gather();
    asm volatile("s_mov_b32 m0, %0\n\ts_nop 0\n\tglobal_load_lds_dword %1, %2" :: "s"(L_KVT + wave * 256), "v"(tv), "s"(tp) : "memory");
    wait_bar(16, 8);
    const rsrc_t rW2 = mkrsrc(w2, F_ * D_ * 2);
    auto wdma = [&](int k) {
      u32 v[5];
#pragma unroll
      for (int i = 0; i < 5; ++i) v[i] = (k == 2 && lo) || (k == 5 && !lo) ? vB[i] : vA[i];
      asm volatile("s_mov_b32 m0, %[m]\n\t" NOP LD(0, s) M0A(1024) NOP LD(1, s) M0A(1024) NOP LD(2, s) M0A(1024) NOP LD(3, s) M0A(1024) NOP LD(4, s)
                   :: [v0] "v"(v[0]), [v1] "v"(v[1]), [v2] "v"(v[2]), [v3] "v"(v[3]), [v4] "v"(v[4]), [rs] "s"(rW2), [s] "s"(so2[k]), [m] "s"((4 * k + a) * 5120) : "memory", "scc"); };
    if (R1F) wdma(0);
    if (R1F == 3) wdma(1);
    if (R1F >= 2) wait_bar(R1F == 3 ? 15 : 10, 9);
    wait_vm<R1F == 3 ? 11 : R1F ? 6 : 1>();
#pragma unroll 1
    for (u32 spins = 0; spins < kSpinCap; ++spins) {
      pin(gc[0]); pin(gc[1]); pin(gs[0]); pin(gs[1]);
      const bool bad = (cson && (gc[0].y != tag32 || gc[0].w != tag32 || gc[1].y != tag32 || gc[1].w != tag32)) ||
                       gs[0].y != tag32 || gs[0].w != tag32 || gs[1].y != tag32 || gs[1].w != tag32;
      if (__builtin_amdgcn_ballot_w64(bad) == 0) break;
      gather(); wait_vm<0>();
    }
    { sts32(L_CS + (64 * a + lane) * 4, __float_as_uint((__uint_as_float(gc[0].x) + __uint_as_float(gc[0].z)) + (__uint_as_float(gc[1].x) + __uint_as_float(gc[1].z))));
      const float v = (__uint_as_float(gs[0].x) + __uint_as_float(gs[0].z)) + (__uint_as_float(gs[1].x) + __uint_as_float(gs[1].z)), w = dpp<0xB1>(v);
      const float mean = v * (1.f / D_), var = max2f(w * (1.f / D_) - mean * mean, 0.f);
      const float inv = __builtin_amdgcn_rcpf(rbf(__builtin_amdgcn_sqrtf(rbf(rbf(var) + 1e-5f))));
      if (!(lane & 1)) sts64(L_ST + (64 * a + lane) * 4, (u32x2){__float_as_uint(inv), __float_as_uint(-rbf(mean) * inv)}); }
    if (!R1F) wdma(0);
    if (R1F < 2) wait_bar(6, 9);
    if (R1F != 3) wdma(1);
    wdma(2);
    lbar();
    STAMP(4);
    bf16x8 fw[15];
#pragma unroll
    for (int i = 0; i < 15; ++i) fw[i] = tr16(fa[2 * (i % 3)] + 8 * (i / 3), fa[2 * (i % 3) + 1] + 8 * (i / 3));
    wdma(3);
    if (isQ) { wdma(4); wdma(5); }
    f32x32 t0, t1, t2; f32x4 t3; const u32 l16 = lane * 16;
    asm volatile("" : "={v[156:159]}"(t3), "={v[160:191]}"(t2), "={v[192:223]}"(t1), "={v[224:255]}"(t0));
#define RL(r, i) "buffer_load_dwordx4 v[" #r "+4*" #i ":" #r "+4*" #i "+3], %[v" #i "], %[rs], %[s] offen\n\t"
#define RLK(txt, V, u) asm volatile("s_cmp_lg_u32 %[q], 0\n\ts_cbranch_scc1 1f\n\t" txt "1:" :: [v0] "v"(V[0]), [v1] "v"(V[1]), [v2] "v"(V[2]), [v3] "v"(V[3]), [v4] "v"(V[4]), \
                                    [rs] "s"(rW2), [s] "s"(so2[u]), [q] "s"(isQ) : "memory", "scc")
    { u32 v[5];
#pragma unroll
      for (int i = 0; i < 5; ++i) v[i] = lo ? vA[i] : vB[i];
      RLK(RL(156, 0) RL(156, 1) RL(156, 2) RL(156, 3) RL(156, 4), vA, 4);
      RLK(RL(176, 0) RL(176, 1) RL(176, 2) RL(176, 3) RL(176, 4), v, 5); }
#pragma unroll
    for (int i = 0; i < 15; ++i) pin(fw[i]);
#define WL(r, i) "buffer_load_dwordx4 v[" #r "+4*" #i ":" #r "+4*" #i "+3], %[p], %[rs], %[s] offen offset:" #i "*1024 sc1\n\t"
#define WL4(r) WL(r, 0) WL(r, 1) WL(r, 2) WL(r, 3) "s_add_u32 %[s], %[s], 4096\n\t"
#define RL5(r) RL(r, 0) RL(r, 1) RL(r, 2) RL(r, 3) RL(r, 4) "s_add_u32 %[s], %[s], 40960\n\t"
    auto oload = [&]() {
      if (TILE && KT) { u32 so = WS_WO + (isQ * 8 + hq) * 102400 + a * 25600;
        asm volatile(WL4(156) WL4(172) WL4(188) WL4(204) WL4(220) WL4(236) WL(252, 0) : [s] "+s"(so) : [p] "v"(l16), [rs] "s"(rWS) : "memory", "scc"); }
      else { u32 so = a * 204800 + n2 * 160, v[5];
#pragma unroll
        for (int i = 0; i < 5; ++i) v[i] = (((64 * i + lane) * 205) >> 11) * 1120 + 1024 * i + l16;
        asm volatile(RL5(156) RL5(176) RL5(196) RL5(216) RL5(236) : [s] "+s"(so)
                     : [v0] "v"(v[0]), [v1] "v"(v[1]), [v2] "v"(v[2]), [v3] "v"(v[3]), [v4] "v"(v[4]), [rs] "s"(rWo) : "memory", "scc"); } };
#define WS(r, o) "ds_write_b128 %[p], v[" #r ":" #r "+3] offset:" #o "\n\t"
#define WS5(r, o) WS(r, o) WS(r+4, o+1024) WS(r+8, o+2048) WS(r+12, o+3072) WS(r+16, o+4096)
#define WST "{v[156:159]}"(t3), "{v[160:191]}"(t2), "{v[192:223]}"(t1), "{v[224:255]}"(t0), [c] "v"(C_W + a * 4)
    auto wflush = [&]() { asm volatile(WS5(156, 0) WS5(176, 5120) WS5(196, 10240) WS5(216, 15360) WS5(236, 20480) "ds_write_b32 %[c], %[k]"
                                       :: WST, [p] "v"(a * 25600 + l16), [k] "v"(4u) : "memory"); };
    auto wfree = [&](u32 k = 2) { return __builtin_amdgcn_ballot_w64(__hip_atomic_load((LAS u32*)(C_P + p4 * 4), __ATOMIC_RELAXED, __HIP_MEMORY_SCOPE_WORKGROUP) < k) == 0; };
    auto wwait = [&](u32 k = 2) {
#pragma unroll 1
      for (u32 spins = 0; spins < kSpinCap && !wfree(k); ++spins) __builtin_amdgcn_s_sleep(1);
      asm volatile("" ::: "memory"); };
      const u32 hb = hq * 4 + a, pb = WS_PART + (hb * 16 + mt) * PART_STRIDE;
      u32 pb0 = pb + lane * 16, pb1 = c16 < 6 ? pb + 1024 + c16 * 64 + g * 16 : OOB, pb2 = g < 2 || c16 < 8 ? pb + 1408 + (g >> 1) * 256 + c16 * 16 + (g & 1) * 8 : OOB;
      pin(pb0); pin(pb1); pin(pb2);
      const u32 ck = mt * 8 + (c16 & 7);
      u32 vo = mt < 14 ? WS_PART + (hb * 16 + 2 * g + (c16 >> 3)) * PART_STRIDE + ck * 16 : OOB;
      u32 rs = mt < 14 && g < 2 && c16 < 8 ? WS_RED + hb * 2048 + ck * 16 + 8 * g : OOB;
      pin(vo); pin(rs);
      u32 vo2 = vo + 8 * PART_STRIDE, fl = (16 + a) * 5120 + l16; pin(vo2); pin(fl);
      const u32 r0 = L_KI + trow * 480; u32 pa = r0 + 80 * a + 16 * p4, pe = r0 + 80 * a + 64 + 8 * (p4 & 1), vm = r0 + 320 + 40 * a + 8 * p4, vxx = r0 + 352 + 40 * a, fwa = brow + 80 * g;
      pin(pa); pin(pe); pin(vm); pin(vxx); pin(fwa);
    __builtin_amdgcn_s_setprio(HPR);
    lbar();
    STAMP(5);
    u32x4 b[2][3];
#pragma unroll
    for (int j = 0; j < 2; ++j) { const u32 r = brow + j * bst; b[j][0] = lds128(r + 16 * g); b[j][1] = lds128(r + 64 + 16 * g); b[j][2] = lds128(r + 128 + 16 * (g & 1)); }
    u32 ph[2][20]; f32x4 pj[2][5];
    project(b[0], fw, pj[0]); project(b[1], fw, pj[1]);
    auto fwrite = [&](int j) {
#pragma unroll
      for (int i = 0; i < 5; ++i) sts128(fwa + j * 7680 + 16 * i, (u32x4){ph[j][4 * i], ph[j][4 * i + 1], ph[j][4 * i + 2], ph[j][4 * i + 3]}); };
    feature_map(pj[0], ph[0], [](int, float&) {});
    if (FW && __builtin_expect(!isQ, 1)) fwrite(0);
    feature_map(pj[1], ph[1], [](int, float&) {});
#pragma unroll
    for (int f = 0; f < 20; ++f) { pin(ph[0][f]); pin(ph[1][f]); }
    STAMP(6);
    const f32x4 z4 = {0.f, 0.f, 0.f, 0.f};
    if (__builtin_expect(!isQ, 1)) {
      if (!FW) fwrite(0);
      fwrite(1);
      lds_arrive(C_KV);
      STAMP(16);
      if (KVO == 2) { wait_vm<10>(); sts_now(C_W + a * 4, 1u); }
      if (!KVO) { RLK(RL(196, 0) RL(196, 1) RL(196, 2) RL(196, 3) RL(196, 4), vA, 6);
        wait_vm<15>();
        sts_now(C_W + a * 4, 1u); }
      lds_wait(C_V, 4);
      bf16x8 v0[4]; u32x4 v1[4];
#pragma unroll
      for (int ks = 0; ks < 4; ++ks) { const u32 o = ks * 32 * 480;
        v0[ks] = tr16(vm + o, vm + o + 7680); v1[ks] = __builtin_bit_cast(u32x4, tr16(vxx + o, vxx + o + 7680));
#pragma unroll
        for (int e = 0; e < 4; ++e) v1[ks][e] = c16 < 4 ? v1[ks][e] : 0x3F803F80u; }
      bf16x8 slo = selector(c16); pin(slo);
#pragma unroll
      for (int ks = 0; ks < 4; ++ks) { pin(v0[ks]); pin(v1[ks]); }
      lds_wait(C_KV, 4);
      STAMP(7);
      f32x4 kv[3][2] = {{z4, z4}, {z4, z4}, {z4, z4}};
#pragma unroll
      for (int ks = 0; ks < 4; ++ks)
#pragma unroll
        for (int f = 0; f < 3; ++f) { const u32 ad = (f < 2 ? pa + 8 * f : pe) + ks * 32 * 480;
          const bf16x8 pf = tr16(ad, ad + 7680);
          kv[f][0] = mfma32(pf, v0[ks], kv[f][0]); kv[f][1] = mfma32(pf, as8(v1[ks]), kv[f][1]); }
      lds_arrive(C_RD);
      STAMP(17);
#pragma unroll
      for (int m = 0; m < 2; ++m) { const u32x2 u = pack(kv[0][m]), v = pack(kv[1][m]);
        st128((u32x4){u.x, u.y, v.x, v.y}, rWS, m ? pb1 : pb0); }
      st64(pack(g < 2 ? kv[2][0] : kv[2][1]), rWS, pb2);
      STAMP(8);
      u32x4 y0, y1; f32x8 pa1, pb1_, pc1; u32 first = 0;
      if (KVO) {
        asm volatile("" : "={v[132:139]}"(pa1), "={v[140:147]}"(pb1_), "={v[148:155]}"(pc1));
        asm volatile(PISS(148) :: [o0] "v"(vo), [o1] "v"(vo2), [r] "s"(rWS) : "memory");
        if (KVO == 5) {
          lds_wait(C_RD, 4);
          asm volatile("s_waitcnt vmcnt(5)\n\t" WS5(156, 0) WS5(176, 20480) :: WST, [p] "v"(fl) : "memory");
          STAMP(9);
          u32 t, u;
          asm volatile("s_waitcnt vmcnt(0)\n\tv_max3_u32 %[t], v[148+0], v[148+1], v[148+2]\n\tv_max3_u32 %[u], v[148+3], v[148+4], v[148+5]\n\t"
                       "v_max3_u32 %[t], %[t], v[148+6], v[148+7]\n\tv_max_u32 %[t], %[t], %[u]\n\tv_cmp_eq_u32 vcc, -1, %[t]\n\ts_mov_b32 %[f], 1\n\ts_cbranch_vccz 1f\n\t"
                       "s_mov_b32 %[f], 0\n\t" PISS(148) "ds_write_b32 %[c], %[k]\n\t" RL(196, 0) RL(196, 1) RL(196, 2) RL(196, 3) RL(196, 4) "1:"
                       : [t] "=&v"(t), [u] "=&v"(u), [f] "=&s"(first), "+{v[148:155]}"(pc1)
                       : [o0] "v"(vo), [o1] "v"(vo2), [r] "s"(rWS), [c] "v"(C_W + a * 4), [k] "v"(1u), [v0] "v"(vA[0]), [v1] "v"(vA[1]), [v2] "v"(vA[2]), [v3] "v"(vA[3]), [v4] "v"(vA[4]),
                         [rs] "s"(rW2), [s] "s"(so2[6]) : "memory", "vcc");
          SPINS(30, poll_data2(rWS, vo, vo2, y0, y1, pc1));
        } else {
        if (KVO == 4) { wait_vm<15>(); sts_now(C_W + a * 4, 1u); }
        RLK(RL(196, 0) RL(196, 1) RL(196, 2) RL(196, 3) RL(196, 4), vA, 6);
        if (KVO == 1) { wait_vm<20>(); sts_now(C_W + a * 4, 1u); }
        lds_wait(C_RD, 4);
        asm volatile("s_waitcnt vmcnt(10)\n\t" WS5(156, 0) WS5(176, 20480) :: WST, [p] "v"(fl) : "memory");
        STAMP(9);
        SPINS(30, poll_data2(rWS, vo, vo2, y0, y1, pc1)); }
      } else {
      lds_wait(C_RD, 4);
      asm volatile("s_waitcnt vmcnt(3)\n\t" WS5(156, 0) WS5(176, 20480) WS5(196, 40960) "ds_write_b32 %[c], %[k]" :: WST, [p] "v"(fl), [k] "v"(3u) : "memory");
      STAMP(9);
      SPINS(30, poll_data<1, PD1>(rWS, vo, vo2, y0, y1, pa1, pb1_, pc1));
      }
      { f32x4 s = mfma32(slo, as8(y1), mfma32(slo, as8(y0), z4));
#pragma unroll
        for (int e = 0; e < 4; ++e) s[e] += dpp<0x128>(s[e]);
        st64(pack(s), rWS, rs); }
      STAMP(10);
      if (KVO == 5 && first) { sts_now(C_W + a * 4, 1u); RLK(RL(196, 0) RL(196, 1) RL(196, 2) RL(196, 3) RL(196, 4), vA, 6); wait_vm<0>(); }
      if (KVO) asm volatile("s_waitcnt vmcnt(1)\n\t" WS5(196, 40960) "ds_write_b32 %[c], %[k]" :: WST, [p] "v"(fl), [k] "v"(3u) : "memory");
      __builtin_amdgcn_s_setprio(0);
      lds_arrive(C_F); lds_wait(C_F, 4);
      if (KD) wwait(KD);
      oload();
      wait_vm<0>();
      pin(pa1); pin(pb1_); pin(pc1);
      STAMP(11);
      wwait(); wflush();
      STAMP(12); STAMP_END;
    } else {
      wait_vm<0>();
      sts_now(C_W + a * 4, 1u);
      wdma(6);
      u32 ob = WS_ACT + (2 * mt + (a >> 1)) * ACT_PANEL + (54 + 5 * (hq >> 1)) * 4096 + (a & 1) * 2048;
      u32 oe = ob + 4 * 4096 + (g & 1) * 1024 + (2 * (hq & 1) + (g >> 1)) * 256 + c16 * 16;
      ob += (hq & 1) * 8192 + lane * 16; pin(ob); pin(oe);
      u32 so = WS_WO + (8 + hq) * 102400 + a * 25600;
      auto wl = [&](int i0, int i1) {
#define WLG(i, r) if (i0 <= i && i < i1) asm volatile(WL4(r) : [s] "+s"(so) : [p] "v"(l16), [rs] "s"(rWS) : "memory", "scc");
        WLG(0, 156) WLG(1, 172) WLG(2, 188) WLG(3, 204) WLG(4, 220) WLG(5, 236)
        if (i0 <= 6 && 6 < i1) asm volatile(WL(252, 0) :: [s] "s"(so), [p] "v"(l16), [rs] "s"(rWS) : "memory"); };
      if (FV == 0) { const u32x4 e = {~0u, ~0u, ~0u, ~0u};
#pragma unroll
        for (int i = 0; i < 4; ++i) st128(e, rWS, ob + (i >> 1) * 4096 + (i & 1) * 1024);
        st128(e, rWS, oe); }
      wl(0, QE);
      wait_vm<4 * QE + (FV ? 0 : 5)>();
      sts_now(C_W + a * 4, 2u);
      if (FV == 0 && !HN) publish<4 * QE>(C_F, rWS, lane < 16 ? WS_OF + (mt * 16 + lane) * 128 + hq * 8 : OOB, tag);
      wwait(1);
      if (FV) put_flag(rWS, q4 == a && g == 0 ? WS_OF + (mt * 16 + lane) * 128 + hq * 8 : OOB, tag);
      wl(QE, QP);
      STAMP(7);
      __builtin_amdgcn_s_setprio(3);
      u32 r0 = WS_RED + (hq * 4 + a) * 2048 + lane * 16, r1 = lane < 48 ? r0 + 1024 : OOB;
      const u32 ta = q4 == g ? L_KVT + g * 1680 + p4 * 80 : L_ZERO, td = q4 == g ? L_KVT + g * 1680 + 1600 : L_ZERO;
      u32 tw = L_KVT + a * 1680 + c16 * 80 + g * 16, tx = L_KVT + a * 1680 + (lane < 24 ? 1280 + (lane >> 2) * 80 + p4 * 16 : (lane - 24) * 80 + 64);
      u32 ofo = lane < 16 ? WS_OF + (mt * 16 + lane) * 128 + hq * 8 : OOB;
      pin(r0); pin(r1); pin(tw); pin(tx); pin(ofo);
      u32x4 v0, v1; u32 spins = 0;
#pragma unroll 1
      for (; spins < kSpinCap; ++spins) {
        v0 = ld128c(rWS, r0, 0); v1 = ld128c(rWS, r1, 0);
        wait_vm<0>(); pin(v0); pin(v1);
        if (__builtin_amdgcn_ballot_w64(umax3(umax3(umax3(v0.x, v0.y, v0.z), v0.w, v1.x), umax3(v1.y, v1.z, v1.w), 0u) == ~0u) == 0) break;
        __builtin_amdgcn_s_sleep(PD2); }
      SPINS(31, spins);
      wl(QP, QM);
      STAMP(11);
      sts128(tw, v0);
      if (lane < 20 || (lane >= 24 && lane < 45)) sts128(tx, v1);
      lds_arrive(C_KV); lds_wait(C_KV, 4);
      STAMP(8);
      f32x4 o[2][6];
#pragma unroll
      for (int dq = 5; dq >= 0; --dq) {
#pragma unroll
        for (int i = 0; i < 5; ++i) { const bf16x8 t = as8(lds128((dq == 5 ? td : ta + 320 * dq) + 16 * i));
#pragma unroll
          for (int j = 0; j < 2; ++j) o[j][dq] = mfma32(t, mk8(ph[j][4 * i], ph[j][4 * i + 1], ph[j][4 * i + 2], ph[j][4 * i + 3]), i ? o[j][dq] : z4);
        } }
      STAMP(16);
      u32x2 ex[2];
#pragma unroll
      for (int j = 0; j < 2; ++j) { const float r = __builtin_amdgcn_rcpf(o[j][5][0] + 1e-6f);
        ex[j] = pack(o[j][4] * r);
#pragma unroll
        for (int p = 1; p >= 0; --p) { const u32x2 u = pack(o[j][2 * p] * r), v = pack(o[j][2 * p + 1] * r);
          st128((u32x4){u.x, u.y, v.x, v.y}, rWS, ob + p * 4096 + j * 1024); } }
      const u32x2 sx2 = sw16(ex[0].x, ex[1].x), sy2 = sw16(ex[0].y, ex[1].y);
      st128((u32x4){sx2.x, sy2.x, sx2.y, sy2.y}, rWS, oe);
      STAMP(9);
      if (HN) publish<63>(C_F, rWS, ofo, tag);
      wl(QM, 7);
      sts_now(C_W + a * 4, 3u);
      STAMP(10);
      if (QP < 7) wait_vm<QM < 7 ? 0 : 5>();
      wwait();
      wflush();
      STAMP(12); STAMP_END;
    }
  } else {
    const int lane = lane_id(), g = lane >> 4, c16 = lane & 15, q4 = c16 >> 2, p4 = lane & 3;
    const u32 trow = 4 * g + q4;
    const int tid = wave * 64 + lane;
    dma16(mkrsrc(wphi + hq * 1600, 3200), (u32)(tid * 16), 0, L_RAW + wave * 1024);
    u32 gb, gb2 = ld16h(mkrsrc(b2 + n2 * 80, 160), tid * 2);
    { const int ti = tid >> 4, c = ti >= 7, t = ti - 7 * c;
      const u32 o = isQ ? 320 * hq + (t == 2 ? 128 + 32 * c : 64 * c + 32 * t + (t < 2 ? 0 : 96)) : 2400 + 128 * hq + 64 * c + 32 * t;
      gb = ld16h(mkrsrc(b1, F_ * 2), (isQ ? t < 5 : t == 5 || t == 6) ? o + 2 * c16 : OOB); }
    if (tid < 16) sts32(L_CTR + tid * 4, 0u);
    if (tid < 108) sts128(L_ZERO + tid * 16, (u32x4){0u, 0u, 0u, 0u});
    mktag();
    u32 va = lane * 16 + wave * 1024, pw = C_W + p4 * 4, po = C_P + wave * 4, fa = WS_HF + ((mt * 4 + kq) * 4 + n4) * 128 + p4 * 8;
    u32 wr = trow * 320 + 8 * p4 + 32 * (g & 1);
    pin(va); pin(pw); pin(po); pin(fa); pin(wr);
    wait_vm<0>(); pin(gb); pin(gb2);
    {
      const u32 pb = WS_PART + ((hq * 4 + wave) * 16 + mt) * PART_STRIDE + lane * 16; const u32x4 e = {~0u, ~0u, ~0u, ~0u};
      st128(e, rWS, !isQ ? pb : lane < 8 ? WS_RED + (hq * 4 + wave) * 2048 + mt * 128 + lane * 16 : OOB);
      if (!isQ) st128(e, rWS, lane < 48 ? pb + 1024 : OOB);
    }
    sts32(L_BB + tid * 4, gb << 16); sts32(L_B2 + tid * 4, gb2 << 16);
    u32 hf;
    if (isQ) gemm1<1>(ws, rWS, tag, lane, wave, mt, hq, hf STAMP_PASS); else gemm1<0>(ws, rWS, tag, lane, wave, mt, hq, hf STAMP_PASS);
    f32x32 r0, r1; f32x16 r2;
    { u32 so1 = WS_ACT + (2 * mt + kh) * ACT_PANEL + 14 * kq * 4096, so2 = WS_ACT + (2 * mt + 1 - kh) * ACT_PANEL + 14 * kq * 4096, cnt;
      const u32 inc = kq == 3 ? OOB : 0x1000, wx = wr ^ 32;
      u32 w0 = kq >= 2 ? wx + 128 : wr, w1 = kq >= 2 ? wr + 192 : wx, w2 = kq >= 2 ? wr : wx + 128, w3 = kq >= 2 ? wx : wr + 192;
      const u32 me = ((mt * 2 + kh) * 8 + n2) * 4 + wave, x1 = WS_XCH + (me ^ 4) * 7680 + 2048 - wave * 1024, x2 = WS_XCH + (me ^ 36) * 7680 + 4096 - wave * 1024;
      u32 l8 = lane * 8 + wave * 1024 + 3072;
      pin(w0); pin(w1); pin(w2); pin(w3); pin(l8);
      if (HF != 1 || isQ) publish<0>(C_H, rWS, hf, tag);
      if (FV && isQ) {
        const u32 ob = WS_ACT + (2 * mt + (wave >> 1)) * ACT_PANEL + (54 + 5 * (hq >> 1)) * 4096 + (wave & 1) * 2048; const u32x4 e = {~0u, ~0u, ~0u, ~0u};
#pragma unroll
        for (int i = 0; i < 4; ++i) st128(e, rWS, ob + (2 * (hq & 1) + (i >> 1)) * 4096 + (i & 1) * 1024 + lane * 16);
        st128(e, rWS, ob + 4 * 4096 + (g & 1) * 1024 + (2 * (hq & 1) + (g >> 1)) * 256 + c16 * 16); }
      AST_DECL
      asm volatile(
      AST(0)
      "s_mov_b32 %[cnt], 0x40000\n\t"
      "buffer_load_dwordx2 v[224:225], %[fa], %[rA], 0 offen sc1\n\t"
      "s_sleep 3\n\t"
      "buffer_load_dwordx2 v[226:227], %[fa], %[rA], 0 offen sc1\n\t"
      "s_sleep 3\n\t"
      "buffer_load_dwordx2 v[228:229], %[fa], %[rA], 0 offen sc1\n\t"
      "6:\n\t"
      "s_waitcnt vmcnt(2)\n\t"
      "v_cmp_ne_u64 vcc, %[tag], v[224:225]\n\t"
      "s_cbranch_vccz 7f\n\t"
      "buffer_load_dwordx2 v[224:225], %[fa], %[rA], 0 offen sc1\n\t"
      "s_waitcnt vmcnt(2)\n\t"
      "v_cmp_ne_u64 vcc, %[tag], v[226:227]\n\t"
      "s_cbranch_vccz 7f\n\t"
      "buffer_load_dwordx2 v[226:227], %[fa], %[rA], 0 offen sc1\n\t"
      "s_waitcnt vmcnt(2)\n\t"
      "v_cmp_ne_u64 vcc, %[tag], v[228:229]\n\t"
      "s_cbranch_vccz 7f\n\t"
      "buffer_load_dwordx2 v[228:229], %[fa], %[rA], 0 offen sc1\n\t"
      "s_sub_u32 %[cnt], %[cnt], 1\n\t"
      "s_cbranch_scc0 6b\n\t"
      "7:\n\t"
      AST(1)
      "s_cmp_lg_u32 %[isq], 0\n\t"
      "s_cbranch_scc1 5f\n\t"
      "s_mov_b32 %[cnt], 0x100000\n\t"
      "4:\n\t"
      "ds_read_b32 v232, %[pw] offset:0\n\t"
      "s_sub_u32 %[cnt], %[cnt], 1\n\t"
      "s_cbranch_scc1 5f\n\t"
      "s_waitcnt lgkmcnt(0)\n\t"
      "v_cmp_gt_u32 vcc, 1, v232\n\t"
      "s_cbranch_vccnz 4b\n\t"
      "5:\n\t"
      "buffer_load_dwordx4 v[120:123], %[va], %[rA], %[so1] offen sc1\n\t"
      "buffer_load_dwordx4 v[124:127], %[va], %[rA], %[so2] offen sc1\n\t"
      "s_add_u32 %[so1], %[so1], 0x1000\n\t"
      "s_add_u32 %[so2], %[so2], 0x1000\n\t"
      "buffer_load_dwordx4 v[128:131], %[va], %[rA], %[so1] offen sc1\n\t"
      "buffer_load_dwordx4 v[132:135], %[va], %[rA], %[so2] offen sc1\n\t"
      "s_add_u32 %[so1], %[so1], 0x1000\n\t"
      "s_add_u32 %[so2], %[so2], 0x1000\n\t"
      "buffer_load_dwordx4 v[136:139], %[va], %[rA], %[so1] offen sc1\n\t"
      "buffer_load_dwordx4 v[140:143], %[va], %[rA], %[so2] offen sc1\n\t"
      "s_add_u32 %[so1], %[so1], 0x1000\n\t"
      "s_add_u32 %[so2], %[so2], 0x1000\n\t"
      "buffer_load_dwordx4 v[144:147], %[va], %[rA], %[so1] offen sc1\n\t"
      "buffer_load_dwordx4 v[148:151], %[va], %[rA], %[so2] offen sc1\n\t"
      "s_add_u32 %[so1], %[so1], 0x1000\n\t"
      "s_add_u32 %[so2], %[so2], 0x1000\n\t"
      "s_mov_b32 %[cnt], 0x100000\n\t"
      "4:\n\t"
      "ds_read_b32 v232, %[pw] offset:0\n\t"
      "s_sub_u32 %[cnt], %[cnt], 1\n\t"
      "s_cbranch_scc1 5f\n\t"
      "s_waitcnt lgkmcnt(0)\n\t"
      "v_cmp_gt_u32 vcc, 1, v232\n\t"
      "s_cbranch_vccnz 4b\n\t"
      "5:\n\t"
      AST(2)
      "ds_read_b64_tr_b16 v[80:81], %[w0] offset:0\n\t"
      "ds_read_b64_tr_b16 v[82:83], %[w0] offset:5120\n\t"
      "ds_read_b64_tr_b16 v[84:85], %[w1] offset:0\n\t"
      "ds_read_b64_tr_b16 v[86:87], %[w1] offset:5120\n\t"
      "ds_read_b64_tr_b16 v[88:89], %[w0] offset:64\n\t"
      "ds_read_b64_tr_b16 v[90:91], %[w0] offset:5184\n\t"
      "ds_read_b64_tr_b16 v[92:93], %[w1] offset:64\n\t"
      "ds_read_b64_tr_b16 v[94:95], %[w1] offset:5184\n\t"
      "ds_read_b64_tr_b16 v[96:97], %[w0] offset:128\n\t"
      "ds_read_b64_tr_b16 v[98:99], %[w0] offset:5248\n\t"
      "ds_read_b64_tr_b16 v[100:101], %[w2] offset:0\n\t"
      "ds_read_b64_tr_b16 v[102:103], %[w2] offset:5120\n\t"
      "ds_read_b64_tr_b16 v[104:105], %[w3] offset:0\n\t"
      "ds_read_b64_tr_b16 v[106:107], %[w3] offset:5120\n\t"
      "ds_read_b64_tr_b16 v[108:109], %[w2] offset:64\n\t"
      "ds_read_b64_tr_b16 v[110:111], %[w2] offset:5184\n\t"
      "s_setprio 0\n\t"
      "s_waitcnt vmcnt(6) lgkmcnt(12)\n\t"
      AST(3)
      "v_mfma_f32_16x16x32_bf16 v[0:3], v[80:83], v[120:123], 0\n\t"
      "ds_read_b64_tr_b16 v[112:113], %[w3] offset:64\n\t"
      "ds_read_b64_tr_b16 v[114:115], %[w3] offset:5184\n\t"
      "buffer_load_dwordx4 v[152:155], %[va], %[rA], %[so1] offen sc1\n\t"
      "v_mfma_f32_16x16x32_bf16 v[40:43], v[80:83], v[124:127], 0\n\t"
      "buffer_load_dwordx4 v[156:159], %[va], %[rA], %[so2] offen sc1\n\t"
      "s_add_u32 %[so1], %[so1], 0x1000\n\t"
      "s_add_u32 %[so2], %[so2], 0x1000\n\t"
      "v_mfma_f32_16x16x32_bf16 v[4:7], v[84:87], v[120:123], 0\n\t"
      "ds_read_b64_tr_b16 v[116:117], %[w2] offset:128\n\t"
      "v_mfma_f32_16x16x32_bf16 v[44:47], v[84:87], v[124:127], 0\n\t"
      "ds_read_b64_tr_b16 v[118:119], %[w2] offset:5248\n\t"
      "s_waitcnt lgkmcnt(12)\n\t"
      "v_mfma_f32_16x16x32_bf16 v[8:11], v[88:91], v[120:123], 0\n\t"
      "ds_read_b64_tr_b16 v[80:81], %[w0] offset:10240\n\t"
      "v_mfma_f32_16x16x32_bf16 v[48:51], v[88:91], v[124:127], 0\n\t"
      "ds_read_b64_tr_b16 v[82:83], %[w0] offset:15360\n\t"
      "v_mfma_f32_16x16x32_bf16 v[12:15], v[92:95], v[120:123], 0\n\t"
      "ds_read_b64_tr_b16 v[84:85], %[w1] offset:10240\n\t"
      "v_mfma_f32_16x16x32_bf16 v[52:55], v[92:95], v[124:127], 0\n\t"
      "ds_read_b64_tr_b16 v[86:87], %[w1] offset:15360\n\t"
      "s_waitcnt lgkmcnt(12)\n\t"
      "v_mfma_f32_16x16x32_bf16 v[16:19], v[96:99], v[120:123], 0\n\t"
      "ds_read_b64_tr_b16 v[88:89], %[w0] offset:10304\n\t"
      "v_mfma_f32_16x16x32_bf16 v[56:59], v[96:99], v[124:127], 0\n\t"
      "ds_read_b64_tr_b16 v[90:91], %[w0] offset:15424\n\t"
      "v_mfma_f32_16x16x32_bf16 v[20:23], v[100:103], v[120:123], 0\n\t"
      "ds_read_b64_tr_b16 v[92:93], %[w1] offset:10304\n\t"
      "v_mfma_f32_16x16x32_bf16 v[60:63], v[100:103], v[124:127], 0\n\t"
      "ds_read_b64_tr_b16 v[94:95], %[w1] offset:15424\n\t"
      "s_waitcnt lgkmcnt(12)\n\t"
      "v_mfma_f32_16x16x32_bf16 v[24:27], v[104:107], v[120:123], 0\n\t"
      "ds_read_b64_tr_b16 v[96:97], %[w0] offset:10368\n\t"
      "ds_read_b64_tr_b16 v[98:99], %[w0] offset:15488\n\t"
      "buffer_load_dwordx4 v[160:163], %[va], %[rA], %[so1] offen sc1\n\t"
      "v_mfma_f32_16x16x32_bf16 v[64:67], v[104:107], v[124:127], 0\n\t"
      "buffer_load_dwordx4 v[164:167], %[va], %[rA], %[so2] offen sc1\n\t"
      "s_add_u32 %[so1], %[so1], 0x1000\n\t"
      "s_add_u32 %[so2], %[so2], 0x1000\n\t"
      "v_mfma_f32_16x16x32_bf16 v[28:31], v[108:111], v[120:123], 0\n\t"
      "ds_read_b64_tr_b16 v[100:101], %[w2] offset:10240\n\t"
      "v_mfma_f32_16x16x32_bf16 v[68:71], v[108:111], v[124:127], 0\n\t"
      "ds_read_b64_tr_b16 v[102:103], %[w2] offset:15360\n\t"
      "s_waitcnt lgkmcnt(12)\n\t"
      "v_mfma_f32_16x16x32_bf16 v[32:35], v[112:115], v[120:123], 0\n\t"
      "ds_read_b64_tr_b16 v[104:105], %[w3] offset:10240\n\t"
      "v_mfma_f32_16x16x32_bf16 v[72:75], v[112:115], v[124:127], 0\n\t"
      "ds_read_b64_tr_b16 v[106:107], %[w3] offset:15360\n\t"
      "v_mfma_f32_16x16x32_bf16 v[36:39], v[116:119], v[120:123], 0\n\t"
      "ds_read_b64_tr_b16 v[108:109], %[w2] offset:10304\n\t"
      "v_mfma_f32_16x16x32_bf16 v[76:79], v[116:119], v[124:127], 0\n\t"
      "ds_read_b64_tr_b16 v[110:111], %[w2] offset:15424\n\t"
      "s_waitcnt vmcnt(8) lgkmcnt(12)\n\t"
      "v_mfma_f32_16x16x32_bf16 v[0:3], v[80:83], v[128:131], v[0:3]\n\t"
      "ds_read_b64_tr_b16 v[112:113], %[w3] offset:10304\n\t"
      "v_mfma_f32_16x16x32_bf16 v[40:43], v[80:83], v[132:135], v[40:43]\n\t"
      "ds_read_b64_tr_b16 v[114:115], %[w3] offset:15424\n\t"
      "v_mfma_f32_16x16x32_bf16 v[4:7], v[84:87], v[128:131], v[4:7]\n\t"
      "ds_read_b64_tr_b16 v[116:117], %[w2] offset:10368\n\t"
      "v_mfma_f32_16x16x32_bf16 v[44:47], v[84:87], v[132:135], v[44:47]\n\t"
      "ds_read_b64_tr_b16 v[118:119], %[w2] offset:15488\n\t"
      "s_waitcnt lgkmcnt(12)\n\t"
      "v_mfma_f32_16x16x32_bf16 v[8:11], v[88:91], v[128:131], v[8:11]\n\t"
      "ds_read_b64_tr_b16 v[80:81], %[w0] offset:20480\n\t"
      "ds_read_b64_tr_b16 v[82:83], %[w0] offset:25600\n\t"
      "buffer_load_dwordx4 v[168:171], %[va], %[rA], %[so1] offen sc1\n\t"
      "v_mfma_f32_16x16x32_bf16 v[48:51], v[88:91], v[132:135], v[48:51]\n\t"
      "buffer_load_dwordx4 v[172:175], %[va], %[rA], %[so2] offen sc1\n\t"
      "s_add_u32 %[so1], %[so1], 0x1000\n\t"
      "s_add_u32 %[so2], %[so2], 0x1000\n\t"
      "v_mfma_f32_16x16x32_bf16 v[12:15], v[92:95], v[128:131], v[12:15]\n\t"
      "ds_read_b64_tr_b16 v[84:85], %[w1] offset:20480\n\t"
      "v_mfma_f32_16x16x32_bf16 v[52:55], v[92:95], v[132:135], v[52:55]\n\t"
      "ds_read_b64_tr_b16 v[86:87], %[w1] offset:25600\n\t"
      "s_waitcnt lgkmcnt(12)\n\t"
      "v_mfma_f32_16x16x32_bf16 v[16:19], v[96:99], v[128:131], v[16:19]\n\t"
      "ds_read_b64_tr_b16 v[88:89], %[w0] offset:20544\n\t"
      "v_mfma_f32_16x16x32_bf16 v[56:59], v[96:99], v[132:135], v[56:59]\n\t"
      "ds_read_b64_tr_b16 v[90:91], %[w0] offset:25664\n\t"
      "v_mfma_f32_16x16x32_bf16 v[20:23], v[100:103], v[128:131], v[20:23]\n\t"
      "ds_read_b64_tr_b16 v[92:93], %[w1] offset:20544\n\t"
      "v_mfma_f32_16x16x32_bf16 v[60:63], v[100:103], v[132:135], v[60:63]\n\t"
      "ds_read_b64_tr_b16 v[94:95], %[w1] offset:25664\n\t"
      "s_waitcnt lgkmcnt(12)\n\t"
      "v_mfma_f32_16x16x32_bf16 v[24:27], v[104:107], v[128:131], v[24:27]\n\t"
      "ds_read_b64_tr_b16 v[96:97], %[w0] offset:20608\n\t"
      "v_mfma_f32_16x16x32_bf16 v[64:67], v[104:107], v[132:135], v[64:67]\n\t"
      "ds_read_b64_tr_b16 v[98:99], %[w0] offset:25728\n\t"
      "v_mfma_f32_16x16x32_bf16 v[28:31], v[108:111], v[128:131], v[28:31]\n\t"
      "ds_read_b64_tr_b16 v[100:101], %[w2] offset:20480\n\t"
      "v_mfma_f32_16x16x32_bf16 v[68:71], v[108:111], v[132:135], v[68:71]\n\t"
      "ds_read_b64_tr_b16 v[102:103], %[w2] offset:25600\n\t"
      "s_waitcnt lgkmcnt(12)\n\t"
      "v_mfma_f32_16x16x32_bf16 v[32:35], v[112:115], v[128:131], v[32:35]\n\t"
      "ds_read_b64_tr_b16 v[104:105], %[w3] offset:20480\n\t"
      "ds_read_b64_tr_b16 v[106:107], %[w3] offset:25600\n\t"
      "buffer_load_dwordx4 v[176:179], %[va], %[rA], %[so1] offen sc1\n\t"
      "v_mfma_f32_16x16x32_bf16 v[72:75], v[112:115], v[132:135], v[72:75]\n\t"
      "buffer_load_dwordx4 v[180:183], %[va], %[rA], %[so2] offen sc1\n\t"
      "s_add_u32 %[so1], %[so1], 0x1000\n\t"
      "s_add_u32 %[so2], %[so2], 0x1000\n\t"
      "v_mfma_f32_16x16x32_bf16 v[36:39], v[116:119], v[128:131], v[36:39]\n\t"
      "ds_read_b64_tr_b16 v[108:109], %[w2] offset:20544\n\t"
      "v_mfma_f32_16x16x32_bf16 v[76:79], v[116:119], v[132:135], v[76:79]\n\t"
      "ds_read_b64_tr_b16 v[110:111], %[w2] offset:25664\n\t"
      "s_waitcnt vmcnt(10) lgkmcnt(12)\n\t"
      "v_mfma_f32_16x16x32_bf16 v[0:3], v[80:83], v[136:139], v[0:3]\n\t"
      "ds_read_b64_tr_b16 v[112:113], %[w3] offset:20544\n\t"
      "v_mfma_f32_16x16x32_bf16 v[40:43], v[80:83], v[140:143], v[40:43]\n\t"
      "ds_read_b64_tr_b16 v[114:115], %[w3] offset:25664\n\t"
      "v_mfma_f32_16x16x32_bf16 v[4:7], v[84:87], v[136:139], v[4:7]\n\t"
      "ds_read_b64_tr_b16 v[116:117], %[w2] offset:20608\n\t"
      "v_mfma_f32_16x16x32_bf16 v[44:47], v[84:87], v[140:143], v[44:47]\n\t"
      "ds_read_b64_tr_b16 v[118:119], %[w2] offset:25728\n\t"
      "s_waitcnt lgkmcnt(12)\n\t"
      "v_mfma_f32_16x16x32_bf16 v[8:11], v[88:91], v[136:139], v[8:11]\n\t"
      "ds_read_b64_tr_b16 v[80:81], %[w0] offset:30720\n\t"
      "v_mfma_f32_16x16x32_bf16 v[48:51], v[88:91], v[140:143], v[48:51]\n\t"
      "ds_read_b64_tr_b16 v[82:83], %[w0] offset:35840\n\t"
      "v_mfma_f32_16x16x32_bf16 v[12:15], v[92:95], v[136:139], v[12:15]\n\t"
      "ds_read_b64_tr_b16 v[84:85], %[w1] offset:30720\n\t"
      "v_mfma_f32_16x16x32_bf16 v[52:55], v[92:95], v[140:143], v[52:55]\n\t"
      "ds_read_b64_tr_b16 v[86:87], %[w1] offset:35840\n\t"
      "s_waitcnt lgkmcnt(12)\n\t"
      "v_mfma_f32_16x16x32_bf16 v[16:19], v[96:99], v[136:139], v[16:19]\n\t"
      "ds_read_b64_tr_b16 v[88:89], %[w0] offset:30784\n\t"
      "ds_read_b64_tr_b16 v[90:91], %[w0] offset:35904\n\t"
      "buffer_load_dwordx4 v[184:187], %[va], %[rA], %[so1] offen sc1\n\t"
      "v_mfma_f32_16x16x32_bf16 v[56:59], v[96:99], v[140:143], v[56:59]\n\t"
      "buffer_load_dwordx4 v[188:191], %[va], %[rA], %[so2] offen sc1\n\t"
      "s_add_u32 %[so1], %[so1], 0x1000\n\t"
      "s_add_u32 %[so2], %[so2], 0x1000\n\t"
      "v_mfma_f32_16x16x32_bf16 v[20:23], v[100:103], v[136:139], v[20:23]\n\t"
      "ds_read_b64_tr_b16 v[92:93], %[w1] offset:30784\n\t"
      "v_mfma_f32_16x16x32_bf16 v[60:63], v[100:103], v[140:143], v[60:63]\n\t"
      "ds_read_b64_tr_b16 v[94:95], %[w1] offset:35904\n\t"
      "s_waitcnt lgkmcnt(12)\n\t"
      "v_mfma_f32_16x16x32_bf16 v[24:27], v[104:107], v[136:139], v[24:27]\n\t"
      "ds_read_b64_tr_b16 v[96:97], %[w0] offset:30848\n\t"
      "v_mfma_f32_16x16x32_bf16 v[64:67], v[104:107], v[140:143], v[64:67]\n\t"
      "ds_read_b64_tr_b16 v[98:99], %[w0] offset:35968\n\t"
      "v_mfma_f32_16x16x32_bf16 v[28:31], v[108:111], v[136:139], v[28:31]\n\t"
      "ds_read_b64_tr_b16 v[100:101], %[w2] offset:30720\n\t"
      "v_mfma_f32_16x16x32_bf16 v[68:71], v[108:111], v[140:143], v[68:71]\n\t"
      "ds_read_b64_tr_b16 v[102:103], %[w2] offset:35840\n\t"
      "s_waitcnt lgkmcnt(12)\n\t"
      "v_mfma_f32_16x16x32_bf16 v[32:35], v[112:115], v[136:139], v[32:35]\n\t"
      "ds_read_b64_tr_b16 v[104:105], %[w3] offset:30720\n\t"
      "v_mfma_f32_16x16x32_bf16 v[72:75], v[112:115], v[140:143], v[72:75]\n\t"
      "ds_read_b64_tr_b16 v[106:107], %[w3] offset:35840\n\t"
      "v_mfma_f32_16x16x32_bf16 v[36:39], v[116:119], v[136:139], v[36:39]\n\t"
      "ds_read_b64_tr_b16 v[108:109], %[w2] offset:30784\n\t"
      "v_mfma_f32_16x16x32_bf16 v[76:79], v[116:119], v[140:143], v[76:79]\n\t"
      "ds_read_b64_tr_b16 v[110:111], %[w2] offset:35904\n\t"
      "s_waitcnt vmcnt(10) lgkmcnt(12)\n\t"
      "v_mfma_f32_16x16x32_bf16 v[0:3], v[80:83], v[144:147], v[0:3]\n\t"
      "ds_read_b64_tr_b16 v[112:113], %[w3] offset:30784\n\t"
      "ds_read_b64_tr_b16 v[114:115], %[w3] offset:35904\n\t"
      "buffer_load_dwordx4 v[192:195], %[va], %[rA], %[so1] offen sc1\n\t"
      "v_mfma_f32_16x16x32_bf16 v[40:43], v[80:83], v[148:151], v[40:43]\n\t"
      "buffer_load_dwordx4 v[196:199], %[va], %[rA], %[so2] offen sc1\n\t"
      "s_add_u32 %[so1], %[so1], 0x1000\n\t"
      "s_add_u32 %[so2], %[so2], 0x1000\n\t"
      "v_mfma_f32_16x16x32_bf16 v[4:7], v[84:87], v[144:147], v[4:7]\n\t"
      "ds_read_b64_tr_b16 v[116:117], %[w2] offset:30848\n\t"
      "v_mfma_f32_16x16x32_bf16 v[44:47], v[84:87], v[148:151], v[44:47]\n\t"
      "ds_read_b64_tr_b16 v[118:119], %[w2] offset:35968\n\t"
      "s_waitcnt lgkmcnt(12)\n\t"
      "v_mfma_f32_16x16x32_bf16 v[8:11], v[88:91], v[144:147], v[8:11]\n\t"
      "ds_read_b64_tr_b16 v[80:81], %[w0] offset:40960\n\t"
      "v_mfma_f32_16x16x32_bf16 v[48:51], v[88:91], v[148:151], v[48:51]\n\t"
      "ds_read_b64_tr_b16 v[82:83], %[w0] offset:46080\n\t"
      "v_mfma_f32_16x16x32_bf16 v[12:15], v[92:95], v[144:147], v[12:15]\n\t"
      "ds_read_b64_tr_b16 v[84:85], %[w1] offset:40960\n\t"
      "v_mfma_f32_16x16x32_bf16 v[52:55], v[92:95], v[148:151], v[52:55]\n\t"
      "ds_read_b64_tr_b16 v[86:87], %[w1] offset:46080\n\t"
      "s_waitcnt lgkmcnt(12)\n\t"
      "v_mfma_f32_16x16x32_bf16 v[16:19], v[96:99], v[144:147], v[16:19]\n\t"
      "ds_read_b64_tr_b16 v[88:89], %[w0] offset:41024\n\t"
      "v_mfma_f32_16x16x32_bf16 v[56:59], v[96:99], v[148:151], v[56:59]\n\t"
      "ds_read_b64_tr_b16 v[90:91], %[w0] offset:46144\n\t"
      "v_mfma_f32_16x16x32_bf16 v[20:23], v[100:103], v[144:147], v[20:23]\n\t"
      "ds_read_b64_tr_b16 v[92:93], %[w1] offset:41024\n\t"
      "v_mfma_f32_16x16x32_bf16 v[60:63], v[100:103], v[148:151], v[60:63]\n\t"
      "ds_read_b64_tr_b16 v[94:95], %[w1] offset:46144\n\t"
      "s_waitcnt lgkmcnt(12)\n\t"
      "v_mfma_f32_16x16x32_bf16 v[24:27], v[104:107], v[144:147], v[24:27]\n\t"
      "ds_read_b64_tr_b16 v[96:97], %[w0] offset:41088\n\t"
      "ds_read_b64_tr_b16 v[98:99], %[w0] offset:46208\n\t"
      "buffer_load_dwordx4 v[200:203], %[va], %[rA], %[so1] offen sc1\n\t"
      "v_mfma_f32_16x16x32_bf16 v[64:67], v[104:107], v[148:151], v[64:67]\n\t"
      "buffer_load_dwordx4 v[204:207], %[va], %[rA], %[so2] offen sc1\n\t"
      "s_add_u32 %[so1], %[so1], 0x1000\n\t"
      "s_add_u32 %[so2], %[so2], 0x1000\n\t"
      "v_mfma_f32_16x16x32_bf16 v[28:31], v[108:111], v[144:147], v[28:31]\n\t"
      "ds_read_b64_tr_b16 v[100:101], %[w2] offset:40960\n\t"
      "v_mfma_f32_16x16x32_bf16 v[68:71], v[108:111], v[148:151], v[68:71]\n\t"
      "ds_read_b64_tr_b16 v[102:103], %[w2] offset:46080\n\t"
      "s_waitcnt lgkmcnt(12)\n\t"
      "v_mfma_f32_16x16x32_bf16 v[32:35], v[112:115], v[144:147], v[32:35]\n\t"
      "ds_read_b64_tr_b16 v[104:105], %[w3] offset:40960\n\t"
      "v_mfma_f32_16x16x32_bf16 v[72:75], v[112:115], v[148:151], v[72:75]\n\t"
      "ds_read_b64_tr_b16 v[106:107], %[w3] offset:46080\n\t"
      "v_mfma_f32_16x16x32_bf16 v[36:39], v[116:119], v[144:147], v[36:39]\n\t"
      "ds_read_b64_tr_b16 v[108:109], %[w2] offset:41024\n\t"
      "v_mfma_f32_16x16x32_bf16 v[76:79], v[116:119], v[148:151], v[76:79]\n\t"
      "ds_read_b64_tr_b16 v[110:111], %[w2] offset:46144\n\t"
      "s_waitcnt vmcnt(12) lgkmcnt(12)\n\t"
      "v_mfma_f32_16x16x32_bf16 v[0:3], v[80:83], v[152:155], v[0:3]\n\t"
      "ds_read_b64_tr_b16 v[112:113], %[w3] offset:41024\n\t"
      "v_mfma_f32_16x16x32_bf16 v[40:43], v[80:83], v[156:159], v[40:43]\n\t"
      "ds_read_b64_tr_b16 v[114:115], %[w3] offset:46144\n\t"
      "v_mfma_f32_16x16x32_bf16 v[4:7], v[84:87], v[152:155], v[4:7]\n\t"
      "ds_read_b64_tr_b16 v[116:117], %[w2] offset:41088\n\t"
      "v_mfma_f32_16x16x32_bf16 v[44:47], v[84:87], v[156:159], v[44:47]\n\t"
      "ds_read_b64_tr_b16 v[118:119], %[w2] offset:46208\n\t"
      "s_waitcnt lgkmcnt(12)\n\t"
      "v_mfma_f32_16x16x32_bf16 v[8:11], v[88:91], v[152:155], v[8:11]\n\t"
      "ds_read_b64_tr_b16 v[80:81], %[w0] offset:51200\n\t"
      "ds_read_b64_tr_b16 v[82:83], %[w0] offset:56320\n\t"
      "buffer_load_dwordx4 v[208:211], %[va], %[rA], %[so1] offen sc1\n\t"
      "v_mfma_f32_16x16x32_bf16 v[48:51], v[88:91], v[156:159], v[48:51]\n\t"
      "buffer_load_dwordx4 v[212:215], %[va], %[rA], %[so2] offen sc1\n\t"
      "s_add_u32 %[so1], %[so1], %[inc]\n\t"
      "s_add_u32 %[so2], %[so2], %[inc]\n\t"
      "v_mfma_f32_16x16x32_bf16 v[12:15], v[92:95], v[152:155], v[12:15]\n\t"
      "ds_read_b64_tr_b16 v[84:85], %[w1] offset:51200\n\t"
      "v_mfma_f32_16x16x32_bf16 v[52:55], v[92:95], v[156:159], v[52:55]\n\t"
      "ds_read_b64_tr_b16 v[86:87], %[w1] offset:56320\n\t"
      "s_waitcnt lgkmcnt(12)\n\t"
      "v_mfma_f32_16x16x32_bf16 v[16:19], v[96:99], v[152:155], v[16:19]\n\t"
      "ds_read_b64_tr_b16 v[88:89], %[w0] offset:51264\n\t"
      "v_mfma_f32_16x16x32_bf16 v[56:59], v[96:99], v[156:159], v[56:59]\n\t"
      "ds_read_b64_tr_b16 v[90:91], %[w0] offset:56384\n\t"
      "v_mfma_f32_16x16x32_bf16 v[20:23], v[100:103], v[152:155], v[20:23]\n\t"
      "ds_read_b64_tr_b16 v[92:93], %[w1] offset:51264\n\t"
      "v_mfma_f32_16x16x32_bf16 v[60:63], v[100:103], v[156:159], v[60:63]\n\t"
      "ds_read_b64_tr_b16 v[94:95], %[w1] offset:56384\n\t"
      "s_waitcnt lgkmcnt(12)\n\t"
      "v_mfma_f32_16x16x32_bf16 v[24:27], v[104:107], v[152:155], v[24:27]\n\t"
      "ds_read_b64_tr_b16 v[96:97], %[w0] offset:51328\n\t"
      "v_mfma_f32_16x16x32_bf16 v[64:67], v[104:107], v[156:159], v[64:67]\n\t"
      "ds_read_b64_tr_b16 v[98:99], %[w0] offset:56448\n\t"
      "v_mfma_f32_16x16x32_bf16 v[28:31], v[108:111], v[152:155], v[28:31]\n\t"
      "ds_read_b64_tr_b16 v[100:101], %[w2] offset:51200\n\t"
      "v_mfma_f32_16x16x32_bf16 v[68:71], v[108:111], v[156:159], v[68:71]\n\t"
      "ds_read_b64_tr_b16 v[102:103], %[w2] offset:56320\n\t"
      "s_waitcnt lgkmcnt(12)\n\t"
      "v_mfma_f32_16x16x32_bf16 v[32:35], v[112:115], v[152:155], v[32:35]\n\t"
      "ds_read_b64_tr_b16 v[104:105], %[w3] offset:51200\n\t"
      "ds_read_b64_tr_b16 v[106:107], %[w3] offset:56320\n\t"
      "buffer_load_dwordx4 v[216:219], %[va], %[rA], %[so1] offen sc1\n\t"
      "v_mfma_f32_16x16x32_bf16 v[72:75], v[112:115], v[156:159], v[72:75]\n\t"
      "buffer_load_dwordx4 v[220:223], %[va], %[rA], %[so2] offen sc1\n\t"
      "s_add_u32 %[so1], %[so1], 0x1000\n\t"
      "s_add_u32 %[so2], %[so2], 0x1000\n\t"
      "v_mfma_f32_16x16x32_bf16 v[36:39], v[116:119], v[152:155], v[36:39]\n\t"
      "ds_read_b64_tr_b16 v[108:109], %[w2] offset:51264\n\t"
      "v_mfma_f32_16x16x32_bf16 v[76:79], v[116:119], v[156:159], v[76:79]\n\t"
      "ds_read_b64_tr_b16 v[110:111], %[w2] offset:56384\n\t"
      "s_waitcnt vmcnt(14) lgkmcnt(12)\n\t"
      "v_mfma_f32_16x16x32_bf16 v[0:3], v[80:83], v[160:163], v[0:3]\n\t"
      "ds_read_b64_tr_b16 v[112:113], %[w3] offset:51264\n\t"
      "v_mfma_f32_16x16x32_bf16 v[40:43], v[80:83], v[164:167], v[40:43]\n\t"
      "ds_read_b64_tr_b16 v[114:115], %[w3] offset:56384\n\t"
      "v_mfma_f32_16x16x32_bf16 v[4:7], v[84:87], v[160:163], v[4:7]\n\t"
      "ds_read_b64_tr_b16 v[116:117], %[w2] offset:51328\n\t"
      "v_mfma_f32_16x16x32_bf16 v[44:47], v[84:87], v[164:167], v[44:47]\n\t"
      "ds_read_b64_tr_b16 v[118:119], %[w2] offset:56448\n\t"
      "s_waitcnt lgkmcnt(12)\n\t"
      "v_mfma_f32_16x16x32_bf16 v[8:11], v[88:91], v[160:163], v[8:11]\n\t"
      "v_add_u32 %[w0], 0xf000, %[w0]\n\t"
      "v_add_u32 %[w1], 0xf000, %[w1]\n\t"
      "v_mfma_f32_16x16x32_bf16 v[48:51], v[88:91], v[164:167], v[48:51]\n\t"
      "ds_read_b64_tr_b16 v[80:81], %[w0] offset:0\n\t"
      "ds_read_b64_tr_b16 v[82:83], %[w0] offset:5120\n\t"
      "v_mfma_f32_16x16x32_bf16 v[12:15], v[92:95], v[160:163], v[12:15]\n\t"
      "ds_read_b64_tr_b16 v[84:85], %[w1] offset:0\n\t"
      "v_mfma_f32_16x16x32_bf16 v[52:55], v[92:95], v[164:167], v[52:55]\n\t"
      "ds_read_b64_tr_b16 v[86:87], %[w1] offset:5120\n\t"
      "s_waitcnt lgkmcnt(12)\n\t"
      "v_mfma_f32_16x16x32_bf16 v[16:19], v[96:99], v[160:163], v[16:19]\n\t"
      "ds_read_b64_tr_b16 v[88:89], %[w0] offset:64\n\t"
      "ds_read_b64_tr_b16 v[90:91], %[w0] offset:5184\n\t"
      "buffer_load_dwordx4 v[224:227], %[va], %[rA], %[so1] offen sc1\n\t"
      "v_mfma_f32_16x16x32_bf16 v[56:59], v[96:99], v[164:167], v[56:59]\n\t"
      "buffer_load_dwordx4 v[228:231], %[va], %[rA], %[so2] offen sc1\n\t"
      "s_add_u32 %[so1], %[so1], 0x1000\n\t"
      "s_add_u32 %[so2], %[so2], 0x1000\n\t"
      "v_mfma_f32_16x16x32_bf16 v[20:23], v[100:103], v[160:163], v[20:23]\n\t"
      "v_mov_b32 v232, 1\n\t"
      "ds_write_b32 %[po], v232\n\t"
      "v_mfma_f32_16x16x32_bf16 v[60:63], v[100:103], v[164:167], v[60:63]\n\t"
      "ds_read_b64_tr_b16 v[92:93], %[w1] offset:64\n\t"
      "ds_read_b64_tr_b16 v[94:95], %[w1] offset:5184\n\t"
      "s_waitcnt lgkmcnt(12)\n\t"
      "v_mfma_f32_16x16x32_bf16 v[24:27], v[104:107], v[160:163], v[24:27]\n\t"
      "ds_read_b64_tr_b16 v[96:97], %[w0] offset:128\n\t"
      "v_mfma_f32_16x16x32_bf16 v[64:67], v[104:107], v[164:167], v[64:67]\n\t"
      "ds_read_b64_tr_b16 v[98:99], %[w0] offset:5248\n\t"
      "v_mfma_f32_16x16x32_bf16 v[28:31], v[108:111], v[160:163], v[28:31]\n\t"
      "v_add_u32 %[w2], 0xf000, %[w2]\n\t"
      "v_add_u32 %[w3], 0xf000, %[w3]\n\t"
      "v_mfma_f32_16x16x32_bf16 v[68:71], v[108:111], v[164:167], v[68:71]\n\t"
      "ds_read_b64_tr_b16 v[100:101], %[w2] offset:0\n\t"
      "ds_read_b64_tr_b16 v[102:103], %[w2] offset:5120\n\t"
      "s_waitcnt lgkmcnt(12)\n\t"
      "v_mfma_f32_16x16x32_bf16 v[32:35], v[112:115], v[160:163], v[32:35]\n\t"
      "ds_read_b64_tr_b16 v[104:105], %[w3] offset:0\n\t"
      "v_mfma_f32_16x16x32_bf16 v[72:75], v[112:115], v[164:167], v[72:75]\n\t"
      "ds_read_b64_tr_b16 v[106:107], %[w3] offset:5120\n\t"
      "v_mfma_f32_16x16x32_bf16 v[36:39], v[116:119], v[160:163], v[36:39]\n\t"
      "ds_read_b64_tr_b16 v[108:109], %[w2] offset:64\n\t"
      "v_mfma_f32_16x16x32_bf16 v[76:79], v[116:119], v[164:167], v[76:79]\n\t"
      "ds_read_b64_tr_b16 v[110:111], %[w2] offset:5184\n\t"
      "s_waitcnt vmcnt(14) lgkmcnt(12)\n\t"
      "v_mfma_f32_16x16x32_bf16 v[0:3], v[80:83], v[168:171], v[0:3]\n\t"
      "ds_read_b64_tr_b16 v[112:113], %[w3] offset:64\n\t"
      "v_mfma_f32_16x16x32_bf16 v[40:43], v[80:83], v[172:175], v[40:43]\n\t"
      "ds_read_b64_tr_b16 v[114:115], %[w3] offset:5184\n\t"
      "v_mfma_f32_16x16x32_bf16 v[4:7], v[84:87], v[168:171], v[4:7]\n\t"
      "ds_read_b64_tr_b16 v[116:117], %[w2] offset:128\n\t"
      "v_mfma_f32_16x16x32_bf16 v[44:47], v[84:87], v[172:175], v[44:47]\n\t"
      "ds_read_b64_tr_b16 v[118:119], %[w2] offset:5248\n\t"
      "s_waitcnt lgkmcnt(12)\n\t"
      "v_mfma_f32_16x16x32_bf16 v[8:11], v[88:91], v[168:171], v[8:11]\n\t"
      "ds_read_b64_tr_b16 v[80:81], %[w0] offset:10240\n\t"
      "v_mfma_f32_16x16x32_bf16 v[48:51], v[88:91], v[172:175], v[48:51]\n\t"
      "ds_read_b64_tr_b16 v[82:83], %[w0] offset:15360\n\t"
      "v_mfma_f32_16x16x32_bf16 v[12:15], v[92:95], v[168:171], v[12:15]\n\t"
      "ds_read_b64_tr_b16 v[84:85], %[w1] offset:10240\n\t"
      "v_mfma_f32_16x16x32_bf16 v[52:55], v[92:95], v[172:175], v[52:55]\n\t"
      "ds_read_b64_tr_b16 v[86:87], %[w1] offset:15360\n\t"
      "s_waitcnt lgkmcnt(12)\n\t"
      "v_mfma_f32_16x16x32_bf16 v[16:19], v[96:99], v[168:171], v[16:19]\n\t"
      "ds_read_b32 v233, %[pw] offset:0\n\t"
      "ds_read_b64_tr_b16 v[88:89], %[w0] offset:10304\n\t"
      "v_mfma_f32_16x16x32_bf16 v[56:59], v[96:99], v[172:175], v[56:59]\n\t"
      "ds_read_b64_tr_b16 v[90:91], %[w0] offset:15424\n\t"
      "v_mfma_f32_16x16x32_bf16 v[20:23], v[100:103], v[168:171], v[20:23]\n\t"
      "ds_read_b64_tr_b16 v[92:93], %[w1] offset:10304\n\t"
      "v_mfma_f32_16x16x32_bf16 v[60:63], v[100:103], v[172:175], v[60:63]\n\t"
      "ds_read_b64_tr_b16 v[94:95], %[w1] offset:15424\n\t"
      "s_waitcnt lgkmcnt(12)\n\t"
      "v_mfma_f32_16x16x32_bf16 v[24:27], v[104:107], v[168:171], v[24:27]\n\t"
      "ds_read_b64_tr_b16 v[96:97], %[w0] offset:10368\n\t"
      "v_mfma_f32_16x16x32_bf16 v[64:67], v[104:107], v[172:175], v[64:67]\n\t"
      "ds_read_b64_tr_b16 v[98:99], %[w0] offset:15488\n\t"
      "v_mfma_f32_16x16x32_bf16 v[28:31], v[108:111], v[168:171], v[28:31]\n\t"
      "ds_read_b64_tr_b16 v[100:101], %[w2] offset:10240\n\t"
      "v_mfma_f32_16x16x32_bf16 v[68:71], v[108:111], v[172:175], v[68:71]\n\t"
      "ds_read_b64_tr_b16 v[102:103], %[w2] offset:15360\n\t"
      "s_waitcnt lgkmcnt(12)\n\t"
      "v_mfma_f32_16x16x32_bf16 v[32:35], v[112:115], v[168:171], v[32:35]\n\t"
      "ds_read_b64_tr_b16 v[104:105], %[w3] offset:10240\n\t"
      "v_mfma_f32_16x16x32_bf16 v[72:75], v[112:115], v[172:175], v[72:75]\n\t"
      "ds_read_b64_tr_b16 v[106:107], %[w3] offset:15360\n\t"
      "v_mfma_f32_16x16x32_bf16 v[36:39], v[116:119], v[168:171], v[36:39]\n\t"
      "ds_read_b64_tr_b16 v[108:109], %[w2] offset:10304\n\t"
      "v_mfma_f32_16x16x32_bf16 v[76:79], v[116:119], v[172:175], v[76:79]\n\t"
      "ds_read_b64_tr_b16 v[110:111], %[w2] offset:15424\n\t"
      "s_waitcnt vmcnt(12) lgkmcnt(12)\n\t"
      AST(4)
      "v_cmp_gt_u32 vcc, 2, v233\n\t"
      "s_cbranch_vccz 3f\n\t"
      "s_mov_b32 %[cnt], 0x100000\n\t"
      "4:\n\t"
      "ds_read_b32 v233, %[pw] offset:0\n\t"
      "s_sub_u32 %[cnt], %[cnt], 1\n\t"
      "s_cbranch_scc1 5f\n\t"
      "s_waitcnt lgkmcnt(0)\n\t"
      "v_cmp_gt_u32 vcc, 2, v233\n\t"
      "s_cbranch_vccnz 4b\n\t"
      "5:\n\t"
      "3:\n\t"
      "v_mfma_f32_16x16x32_bf16 v[0:3], v[80:83], v[176:179], v[0:3]\n\t"
      "ds_read_b64_tr_b16 v[112:113], %[w3] offset:10304\n\t"
      "v_mfma_f32_16x16x32_bf16 v[40:43], v[80:83], v[180:183], v[40:43]\n\t"
      "ds_read_b64_tr_b16 v[114:115], %[w3] offset:15424\n\t"
      "v_mfma_f32_16x16x32_bf16 v[4:7], v[84:87], v[176:179], v[4:7]\n\t"
      "ds_read_b64_tr_b16 v[116:117], %[w2] offset:10368\n\t"
      "v_mfma_f32_16x16x32_bf16 v[44:47], v[84:87], v[180:183], v[44:47]\n\t"
      "ds_read_b64_tr_b16 v[118:119], %[w2] offset:15488\n\t"
      "s_waitcnt lgkmcnt(12)\n\t"
      "v_mfma_f32_16x16x32_bf16 v[8:11], v[88:91], v[176:179], v[8:11]\n\t"
      "ds_read_b64_tr_b16 v[80:81], %[w2] offset:20480\n\t"
      "v_mfma_f32_16x16x32_bf16 v[48:51], v[88:91], v[180:183], v[48:51]\n\t"
      "ds_read_b64_tr_b16 v[82:83], %[w2] offset:25600\n\t"
      "v_mfma_f32_16x16x32_bf16 v[12:15], v[92:95], v[176:179], v[12:15]\n\t"
      "ds_read_b64_tr_b16 v[84:85], %[w3] offset:20480\n\t"
      "v_mfma_f32_16x16x32_bf16 v[52:55], v[92:95], v[180:183], v[52:55]\n\t"
      "ds_read_b64_tr_b16 v[86:87], %[w3] offset:25600\n\t"
      "s_waitcnt lgkmcnt(12)\n\t"
      "v_mfma_f32_16x16x32_bf16 v[16:19], v[96:99], v[176:179], v[16:19]\n\t"
      "ds_read_b64_tr_b16 v[88:89], %[w2] offset:20544\n\t"
      "v_mfma_f32_16x16x32_bf16 v[56:59], v[96:99], v[180:183], v[56:59]\n\t"
      "ds_read_b64_tr_b16 v[90:91], %[w2] offset:25664\n\t"
      "v_mfma_f32_16x16x32_bf16 v[20:23], v[100:103], v[176:179], v[20:23]\n\t"
      "ds_read_b64_tr_b16 v[92:93], %[w3] offset:20544\n\t"
      "v_mfma_f32_16x16x32_bf16 v[60:63], v[100:103], v[180:183], v[60:63]\n\t"
      "ds_read_b64_tr_b16 v[94:95], %[w3] offset:25664\n\t"
      "s_waitcnt lgkmcnt(12)\n\t"
      "v_mfma_f32_16x16x32_bf16 v[24:27], v[104:107], v[176:179], v[24:27]\n\t"
      "ds_read_b64_tr_b16 v[96:97], %[w2] offset:20608\n\t"
      "v_mfma_f32_16x16x32_bf16 v[64:67], v[104:107], v[180:183], v[64:67]\n\t"
      "ds_read_b64_tr_b16 v[98:99], %[w2] offset:25728\n\t"
      "v_mfma_f32_16x16x32_bf16 v[28:31], v[108:111], v[176:179], v[28:31]\n\t"
      "ds_read_b64_tr_b16 v[100:101], %[w2] offset:30720\n\t"
      "v_mfma_f32_16x16x32_bf16 v[68:71], v[108:111], v[180:183], v[68:71]\n\t"
      "ds_read_b64_tr_b16 v[102:103], %[w2] offset:35840\n\t"
      "s_waitcnt lgkmcnt(12)\n\t"
      "v_mfma_f32_16x16x32_bf16 v[32:35], v[112:115], v[176:179], v[32:35]\n\t"
      "ds_read_b64_tr_b16 v[104:105], %[w3] offset:30720\n\t"
      "v_mfma_f32_16x16x32_bf16 v[72:75], v[112:115], v[180:183], v[72:75]\n\t"
      "ds_read_b64_tr_b16 v[106:107], %[w3] offset:35840\n\t"
      "v_mfma_f32_16x16x32_bf16 v[36:39], v[116:119], v[176:179], v[36:39]\n\t"
      "ds_read_b64_tr_b16 v[108:109], %[w2] offset:30784\n\t"
      "v_mfma_f32_16x16x32_bf16 v[76:79], v[116:119], v[180:183], v[76:79]\n\t"
      "ds_read_b64_tr_b16 v[110:111], %[w2] offset:35904\n\t"
      "s_waitcnt vmcnt(10) lgkmcnt(12)\n\t"
      "v_mfma_f32_16x16x32_bf16 v[20:23], v[80:83], v[184:187], v[20:23]\n\t"
      "ds_read_b64_tr_b16 v[112:113], %[w3] offset:30784\n\t"
      "v_mfma_f32_16x16x32_bf16 v[60:63], v[80:83], v[188:191], v[60:63]\n\t"
      "ds_read_b64_tr_b16 v[114:115], %[w3] offset:35904\n\t"
      "v_mfma_f32_16x16x32_bf16 v[24:27], v[84:87], v[184:187], v[24:27]\n\t"
      "ds_read_b64_tr_b16 v[116:117], %[w2] offset:30848\n\t"
      "v_mfma_f32_16x16x32_bf16 v[64:67], v[84:87], v[188:191], v[64:67]\n\t"
      "ds_read_b64_tr_b16 v[118:119], %[w2] offset:35968\n\t"
      "s_waitcnt lgkmcnt(12)\n\t"
      "v_mfma_f32_16x16x32_bf16 v[28:31], v[88:91], v[184:187], v[28:31]\n\t"
      "ds_read_b64_tr_b16 v[80:81], %[w2] offset:40960\n\t"
      "v_mfma_f32_16x16x32_bf16 v[68:71], v[88:91], v[188:191], v[68:71]\n\t"
      "ds_read_b64_tr_b16 v[82:83], %[w2] offset:46080\n\t"
      "v_mfma_f32_16x16x32_bf16 v[32:35], v[92:95], v[184:187], v[32:35]\n\t"
      "ds_read_b64_tr_b16 v[84:85], %[w3] offset:40960\n\t"
      "v_mfma_f32_16x16x32_bf16 v[72:75], v[92:95], v[188:191], v[72:75]\n\t"
      "ds_read_b64_tr_b16 v[86:87], %[w3] offset:46080\n\t"
      "s_waitcnt vmcnt(8) lgkmcnt(12)\n\t"
      "v_mfma_f32_16x16x32_bf16 v[36:39], v[96:99], v[184:187], v[36:39]\n\t"
      "ds_read_b64_tr_b16 v[88:89], %[w2] offset:41024\n\t"
      "v_mfma_f32_16x16x32_bf16 v[76:79], v[96:99], v[188:191], v[76:79]\n\t"
      "ds_read_b64_tr_b16 v[90:91], %[w2] offset:46144\n\t"
      "v_mfma_f32_16x16x32_bf16 v[20:23], v[100:103], v[192:195], v[20:23]\n\t"
      "ds_read_b64_tr_b16 v[92:93], %[w3] offset:41024\n\t"
      "v_mfma_f32_16x16x32_bf16 v[60:63], v[100:103], v[196:199], v[60:63]\n\t"
      "ds_read_b64_tr_b16 v[94:95], %[w3] offset:46144\n\t"
      "s_waitcnt lgkmcnt(12)\n\t"
      "v_mfma_f32_16x16x32_bf16 v[24:27], v[104:107], v[192:195], v[24:27]\n\t"
      "ds_read_b64_tr_b16 v[96:97], %[w2] offset:41088\n\t"
      "v_mfma_f32_16x16x32_bf16 v[64:67], v[104:107], v[196:199], v[64:67]\n\t"
      "ds_read_b64_tr_b16 v[98:99], %[w2] offset:46208\n\t"
      "v_mfma_f32_16x16x32_bf16 v[28:31], v[108:111], v[192:195], v[28:31]\n\t"
      "ds_read_b64_tr_b16 v[100:101], %[w2] offset:51200\n\t"
      "v_mfma_f32_16x16x32_bf16 v[68:71], v[108:111], v[196:199], v[68:71]\n\t"
      "ds_read_b64_tr_b16 v[102:103], %[w2] offset:56320\n\t"
      "s_waitcnt lgkmcnt(12)\n\t"
      "v_mfma_f32_16x16x32_bf16 v[32:35], v[112:115], v[192:195], v[32:35]\n\t"
      "ds_read_b64_tr_b16 v[104:105], %[w3] offset:51200\n\t"
      "v_mfma_f32_16x16x32_bf16 v[72:75], v[112:115], v[196:199], v[72:75]\n\t"
      "ds_read_b64_tr_b16 v[106:107], %[w3] offset:56320\n\t"
      "v_mfma_f32_16x16x32_bf16 v[36:39], v[116:119], v[192:195], v[36:39]\n\t"
      "ds_read_b64_tr_b16 v[108:109], %[w2] offset:51264\n\t"
      "v_mfma_f32_16x16x32_bf16 v[76:79], v[116:119], v[196:199], v[76:79]\n\t"
      "ds_read_b64_tr_b16 v[110:111], %[w2] offset:56384\n\t"
      "s_waitcnt vmcnt(6) lgkmcnt(12)\n\t"
      "v_mfma_f32_16x16x32_bf16 v[20:23], v[80:83], v[200:203], v[20:23]\n\t"
      "ds_read_b64_tr_b16 v[112:113], %[w3] offset:51264\n\t"
      "v_mfma_f32_16x16x32_bf16 v[60:63], v[80:83], v[204:207], v[60:63]\n\t"
      "ds_read_b64_tr_b16 v[114:115], %[w3] offset:56384\n\t"
      "v_mfma_f32_16x16x32_bf16 v[24:27], v[84:87], v[200:203], v[24:27]\n\t"
      "ds_read_b64_tr_b16 v[116:117], %[w2] offset:51328\n\t"
      "v_mfma_f32_16x16x32_bf16 v[64:67], v[84:87], v[204:207], v[64:67]\n\t"
      "ds_read_b64_tr_b16 v[118:119], %[w2] offset:56448\n\t"
      "s_waitcnt lgkmcnt(12)\n\t"
      "v_mfma_f32_16x16x32_bf16 v[28:31], v[88:91], v[200:203], v[28:31]\n\t"
      "v_add_u32 %[w2], 0xf000, %[w2]\n\t"
      "v_add_u32 %[w3], 0xf000, %[w3]\n\t"
      "v_mfma_f32_16x16x32_bf16 v[68:71], v[88:91], v[204:207], v[68:71]\n\t"
      "ds_read_b64_tr_b16 v[80:81], %[w2] offset:0\n\t"
      "ds_read_b64_tr_b16 v[82:83], %[w2] offset:5120\n\t"
      "v_mfma_f32_16x16x32_bf16 v[32:35], v[92:95], v[200:203], v[32:35]\n\t"
      "ds_read_b64_tr_b16 v[84:85], %[w3] offset:0\n\t"
      "v_mfma_f32_16x16x32_bf16 v[72:75], v[92:95], v[204:207], v[72:75]\n\t"
      "ds_read_b64_tr_b16 v[86:87], %[w3] offset:5120\n\t"
      "s_waitcnt vmcnt(4) lgkmcnt(12)\n\t"
      "v_mfma_f32_16x16x32_bf16 v[36:39], v[96:99], v[200:203], v[36:39]\n\t"
      "ds_read_b64_tr_b16 v[88:89], %[w2] offset:64\n\t"
      "v_mfma_f32_16x16x32_bf16 v[76:79], v[96:99], v[204:207], v[76:79]\n\t"
      "ds_read_b64_tr_b16 v[90:91], %[w2] offset:5184\n\t"
      "v_mfma_f32_16x16x32_bf16 v[20:23], v[100:103], v[208:211], v[20:23]\n\t"
      "ds_read_b64_tr_b16 v[92:93], %[w3] offset:64\n\t"
      "v_mfma_f32_16x16x32_bf16 v[60:63], v[100:103], v[212:215], v[60:63]\n\t"
      "ds_read_b64_tr_b16 v[94:95], %[w3] offset:5184\n\t"
      "s_waitcnt lgkmcnt(12)\n\t"
      "v_mfma_f32_16x16x32_bf16 v[24:27], v[104:107], v[208:211], v[24:27]\n\t"
      "ds_read_b64_tr_b16 v[96:97], %[w2] offset:128\n\t"
      "v_mfma_f32_16x16x32_bf16 v[64:67], v[104:107], v[212:215], v[64:67]\n\t"
      "ds_read_b64_tr_b16 v[98:99], %[w2] offset:5248\n\t"
      "v_mfma_f32_16x16x32_bf16 v[28:31], v[108:111], v[208:211], v[28:31]\n\t"
      "ds_read_b64_tr_b16 v[100:101], %[w2] offset:10240\n\t"
      "v_mfma_f32_16x16x32_bf16 v[68:71], v[108:111], v[212:215], v[68:71]\n\t"
      "ds_read_b64_tr_b16 v[102:103], %[w2] offset:15360\n\t"
      "s_waitcnt lgkmcnt(12)\n\t"
      "v_mfma_f32_16x16x32_bf16 v[32:35], v[112:115], v[208:211], v[32:35]\n\t"
      "ds_read_b64_tr_b16 v[104:105], %[w3] offset:10240\n\t"
      "v_mfma_f32_16x16x32_bf16 v[72:75], v[112:115], v[212:215], v[72:75]\n\t"
      "ds_read_b64_tr_b16 v[106:107], %[w3] offset:15360\n\t"
      "v_mfma_f32_16x16x32_bf16 v[36:39], v[116:119], v[208:211], v[36:39]\n\t"
      "ds_read_b64_tr_b16 v[108:109], %[w2] offset:10304\n\t"
      "v_mfma_f32_16x16x32_bf16 v[76:79], v[116:119], v[212:215], v[76:79]\n\t"
      "ds_read_b64_tr_b16 v[110:111], %[w2] offset:15424\n\t"
      "s_waitcnt vmcnt(2) lgkmcnt(12)\n\t"
      "v_mfma_f32_16x16x32_bf16 v[20:23], v[80:83], v[216:219], v[20:23]\n\t"
      "ds_read_b64_tr_b16 v[112:113], %[w3] offset:10304\n\t"
      "v_mfma_f32_16x16x32_bf16 v[60:63], v[80:83], v[220:223], v[60:63]\n\t"
      "ds_read_b64_tr_b16 v[114:115], %[w3] offset:15424\n\t"
      "v_mfma_f32_16x16x32_bf16 v[24:27], v[84:87], v[216:219], v[24:27]\n\t"
      "ds_read_b64_tr_b16 v[116:117], %[w2] offset:10368\n\t"
      "v_mfma_f32_16x16x32_bf16 v[64:67], v[84:87], v[220:223], v[64:67]\n\t"
      "ds_read_b64_tr_b16 v[118:119], %[w2] offset:15488\n\t"
      "s_waitcnt lgkmcnt(12)\n\t"
      "v_mfma_f32_16x16x32_bf16 v[28:31], v[88:91], v[216:219], v[28:31]\n\t"
      "ds_read_b64_tr_b16 v[80:81], %[w0] offset:20480\n\t"
      "v_mfma_f32_16x16x32_bf16 v[68:71], v[88:91], v[220:223], v[68:71]\n\t"
      "ds_read_b64_tr_b16 v[82:83], %[w0] offset:25600\n\t"
      "v_mfma_f32_16x16x32_bf16 v[32:35], v[92:95], v[216:219], v[32:35]\n\t"
      "ds_read_b64_tr_b16 v[84:85], %[w1] offset:20480\n\t"
      "v_mfma_f32_16x16x32_bf16 v[72:75], v[92:95], v[220:223], v[72:75]\n\t"
      "ds_read_b64_tr_b16 v[86:87], %[w1] offset:25600\n\t"
      "s_waitcnt vmcnt(0) lgkmcnt(12)\n\t"
      "v_mfma_f32_16x16x32_bf16 v[36:39], v[96:99], v[216:219], v[36:39]\n\t"
      "ds_read_b64_tr_b16 v[88:89], %[w0] offset:20544\n\t"
      "v_mfma_f32_16x16x32_bf16 v[76:79], v[96:99], v[220:223], v[76:79]\n\t"
      "ds_read_b64_tr_b16 v[90:91], %[w0] offset:25664\n\t"
      "v_mfma_f32_16x16x32_bf16 v[20:23], v[100:103], v[224:227], v[20:23]\n\t"
      "ds_read_b64_tr_b16 v[92:93], %[w1] offset:20544\n\t"
      "v_mfma_f32_16x16x32_bf16 v[60:63], v[100:103], v[228:231], v[60:63]\n\t"
      "ds_read_b64_tr_b16 v[94:95], %[w1] offset:25664\n\t"
      "s_waitcnt lgkmcnt(12)\n\t"
      "v_mfma_f32_16x16x32_bf16 v[24:27], v[104:107], v[224:227], v[24:27]\n\t"
      "ds_read_b64_tr_b16 v[96:97], %[w0] offset:20608\n\t"
      "v_mfma_f32_16x16x32_bf16 v[64:67], v[104:107], v[228:231], v[64:67]\n\t"
      "ds_read_b64_tr_b16 v[98:99], %[w0] offset:25728\n\t"
      "v_mfma_f32_16x16x32_bf16 v[28:31], v[108:111], v[224:227], v[28:31]\n\t"
      "ds_read_b64_tr_b16 v[100:101], %[w0] offset:30720\n\t"
      "v_mfma_f32_16x16x32_bf16 v[68:71], v[108:111], v[228:231], v[68:71]\n\t"
      "ds_read_b64_tr_b16 v[102:103], %[w0] offset:35840\n\t"
      "s_waitcnt lgkmcnt(12)\n\t"
      "v_mfma_f32_16x16x32_bf16 v[32:35], v[112:115], v[224:227], v[32:35]\n\t"
      "ds_read_b64_tr_b16 v[104:105], %[w1] offset:30720\n\t"
      "v_mfma_f32_16x16x32_bf16 v[72:75], v[112:115], v[228:231], v[72:75]\n\t"
      "ds_read_b64_tr_b16 v[106:107], %[w1] offset:35840\n\t"
      "v_mfma_f32_16x16x32_bf16 v[36:39], v[116:119], v[224:227], v[36:39]\n\t"
      "ds_read_b64_tr_b16 v[108:109], %[w0] offset:30784\n\t"
      "v_mfma_f32_16x16x32_bf16 v[76:79], v[116:119], v[228:231], v[76:79]\n\t"
      "ds_read_b64_tr_b16 v[110:111], %[w0] offset:35904\n\t"
      "s_waitcnt lgkmcnt(12)\n\t"
      "v_mfma_f32_16x16x32_bf16 v[0:3], v[80:83], v[184:187], v[0:3]\n\t"
      "ds_read_b64_tr_b16 v[112:113], %[w1] offset:30784\n\t"
      "v_mfma_f32_16x16x32_bf16 v[40:43], v[80:83], v[188:191], v[40:43]\n\t"
      "ds_read_b64_tr_b16 v[114:115], %[w1] offset:35904\n\t"
      "v_mfma_f32_16x16x32_bf16 v[4:7], v[84:87], v[184:187], v[4:7]\n\t"
      "ds_read_b64_tr_b16 v[116:117], %[w0] offset:30848\n\t"
      "v_mfma_f32_16x16x32_bf16 v[44:47], v[84:87], v[188:191], v[44:47]\n\t"
      "ds_read_b64_tr_b16 v[118:119], %[w0] offset:35968\n\t"
      "s_waitcnt lgkmcnt(12)\n\t"
      "v_mfma_f32_16x16x32_bf16 v[8:11], v[88:91], v[184:187], v[8:11]\n\t"
      "v_mov_b32 v232, 2\n\t"
      "ds_write_b32 %[po], v232\n\t"
      "v_mfma_f32_16x16x32_bf16 v[48:51], v[88:91], v[188:191], v[48:51]\n\t"
      "ds_read_b64_tr_b16 v[80:81], %[w0] offset:40960\n\t"
      "ds_read_b64_tr_b16 v[82:83], %[w0] offset:46080\n\t"
      "v_mfma_f32_16x16x32_bf16 v[12:15], v[92:95], v[184:187], v[12:15]\n\t"
      "ds_read_b64_tr_b16 v[84:85], %[w1] offset:40960\n\t"
      "ds_read_b64_tr_b16 v[86:87], %[w1] offset:46080\n\t"
      "v_mfma_f32_16x16x32_bf16 v[52:55], v[92:95], v[188:191], v[52:55]\n\t"
      "v_cvt_pk_bf16_f32 v120, v20, v21\n\t"
      "v_cvt_pk_bf16_f32 v121, v22, v23\n\t"
      "s_waitcnt lgkmcnt(12)\n\t"
      "v_mfma_f32_16x16x32_bf16 v[16:19], v[96:99], v[184:187], v[16:19]\n\t"
      "ds_read_b64_tr_b16 v[88:89], %[w0] offset:41024\n\t"
      "ds_read_b64_tr_b16 v[90:91], %[w0] offset:46144\n\t"
      "v_mfma_f32_16x16x32_bf16 v[56:59], v[96:99], v[188:191], v[56:59]\n\t"
      "v_cvt_pk_bf16_f32 v122, v24, v25\n\t"
      "v_cvt_pk_bf16_f32 v123, v26, v27\n\t"
      "v_mfma_f32_16x16x32_bf16 v[0:3], v[100:103], v[192:195], v[0:3]\n\t"
      "ds_read_b64_tr_b16 v[92:93], %[w1] offset:41024\n\t"
      "ds_read_b64_tr_b16 v[94:95], %[w1] offset:46144\n\t"
      "v_mfma_f32_16x16x32_bf16 v[40:43], v[100:103], v[196:199], v[40:43]\n\t"
      "v_cvt_pk_bf16_f32 v124, v28, v29\n\t"
      "v_cvt_pk_bf16_f32 v125, v30, v31\n\t"
      "s_waitcnt lgkmcnt(12)\n\t"
      "v_mfma_f32_16x16x32_bf16 v[4:7], v[104:107], v[192:195], v[4:7]\n\t"
      "ds_read_b64_tr_b16 v[96:97], %[w0] offset:41088\n\t"
      "ds_read_b64_tr_b16 v[98:99], %[w0] offset:46208\n\t"
      "v_mfma_f32_16x16x32_bf16 v[44:47], v[104:107], v[196:199], v[44:47]\n\t"
      "v_cvt_pk_bf16_f32 v126, v32, v33\n\t"
      "v_cvt_pk_bf16_f32 v127, v34, v35\n\t"
      "v_mfma_f32_16x16x32_bf16 v[8:11], v[108:111], v[192:195], v[8:11]\n\t"
      "ds_read_b64_tr_b16 v[100:101], %[w0] offset:51200\n\t"
      "ds_read_b64_tr_b16 v[102:103], %[w0] offset:56320\n\t"
      "v_mfma_f32_16x16x32_bf16 v[48:51], v[108:111], v[196:199], v[48:51]\n\t"
      "v_cvt_pk_bf16_f32 v128, v36, v37\n\t"
      "v_cvt_pk_bf16_f32 v129, v38, v39\n\t"
      "s_waitcnt lgkmcnt(12)\n\t"
      "v_mfma_f32_16x16x32_bf16 v[12:15], v[112:115], v[192:195], v[12:15]\n\t"
      "ds_read_b64_tr_b16 v[104:105], %[w1] offset:51200\n\t"
      "ds_read_b64_tr_b16 v[106:107], %[w1] offset:56320\n\t"
      "v_mfma_f32_16x16x32_bf16 v[52:55], v[112:115], v[196:199], v[52:55]\n\t"
      "buffer_store_dwordx4 v[120:123], %[va], %[rA], %[x1] offen sc1\n\t"
      "buffer_store_dwordx4 v[124:127], %[va], %[rA], %[x1] offen offset:1024 sc1\n\t"
      "v_mfma_f32_16x16x32_bf16 v[16:19], v[116:119], v[192:195], v[16:19]\n\t"
      "ds_read_b64_tr_b16 v[108:109], %[w0] offset:51264\n\t"
      "ds_read_b64_tr_b16 v[110:111], %[w0] offset:56384\n\t"
      "v_mfma_f32_16x16x32_bf16 v[56:59], v[116:119], v[196:199], v[56:59]\n\t"
      "buffer_store_dwordx2 v[128:129], %[l8], %[rA], %[x1] offen offset:1536 sc1\n\t"
      "v_cvt_pk_bf16_f32 v132, v60, v61\n\t"
      "s_waitcnt lgkmcnt(12)\n\t"
      "v_mfma_f32_16x16x32_bf16 v[0:3], v[80:83], v[200:203], v[0:3]\n\t"
      "ds_read_b64_tr_b16 v[112:113], %[w1] offset:51264\n\t"
      "ds_read_b64_tr_b16 v[114:115], %[w1] offset:56384\n\t"
      "v_mfma_f32_16x16x32_bf16 v[40:43], v[80:83], v[204:207], v[40:43]\n\t"
      "v_cvt_pk_bf16_f32 v133, v62, v63\n\t"
      "v_cvt_pk_bf16_f32 v134, v64, v65\n\t"
      "v_mfma_f32_16x16x32_bf16 v[4:7], v[84:87], v[200:203], v[4:7]\n\t"
      "ds_read_b64_tr_b16 v[116:117], %[w0] offset:51328\n\t"
      "ds_read_b64_tr_b16 v[118:119], %[w0] offset:56448\n\t"
      "v_mfma_f32_16x16x32_bf16 v[44:47], v[84:87], v[204:207], v[44:47]\n\t"
      "v_cvt_pk_bf16_f32 v135, v66, v67\n\t"
      "v_cvt_pk_bf16_f32 v136, v68, v69\n\t"
      "s_waitcnt lgkmcnt(12)\n\t"
      "v_mfma_f32_16x16x32_bf16 v[8:11], v[88:91], v[200:203], v[8:11]\n\t"
      "v_add_u32 %[w0], 0xf000, %[w0]\n\t"
      "v_add_u32 %[w1], 0xf000, %[w1]\n\t"
      "ds_read_b64_tr_b16 v[80:81], %[w0] offset:0\n\t"
      "v_mfma_f32_16x16x32_bf16 v[48:51], v[88:91], v[204:207], v[48:51]\n\t"
      "ds_read_b64_tr_b16 v[82:83], %[w0] offset:5120\n\t"
      "v_cvt_pk_bf16_f32 v137, v70, v71\n\t"
      "v_cvt_pk_bf16_f32 v138, v72, v73\n\t"
      "v_mfma_f32_16x16x32_bf16 v[12:15], v[92:95], v[200:203], v[12:15]\n\t"
      "ds_read_b64_tr_b16 v[84:85], %[w1] offset:0\n\t"
      "ds_read_b64_tr_b16 v[86:87], %[w1] offset:5120\n\t"
      "v_mfma_f32_16x16x32_bf16 v[52:55], v[92:95], v[204:207], v[52:55]\n\t"
      "v_cvt_pk_bf16_f32 v139, v74, v75\n\t"
      "v_cvt_pk_bf16_f32 v140, v76, v77\n\t"
      "s_waitcnt lgkmcnt(12)\n\t"
      "v_mfma_f32_16x16x32_bf16 v[16:19], v[96:99], v[200:203], v[16:19]\n\t"
      "ds_read_b64_tr_b16 v[88:89], %[w0] offset:64\n\t"
      "ds_read_b64_tr_b16 v[90:91], %[w0] offset:5184\n\t"
      "v_mfma_f32_16x16x32_bf16 v[56:59], v[96:99], v[204:207], v[56:59]\n\t"
      "v_cvt_pk_bf16_f32 v141, v78, v79\n\t"
      "buffer_store_dwordx4 v[132:135], %[va], %[rA], %[x2] offen sc1\n\t"
      "v_mfma_f32_16x16x32_bf16 v[0:3], v[100:103], v[208:211], v[0:3]\n\t"
      "ds_read_b64_tr_b16 v[92:93], %[w1] offset:64\n\t"
      "ds_read_b64_tr_b16 v[94:95], %[w1] offset:5184\n\t"
      "v_mfma_f32_16x16x32_bf16 v[40:43], v[100:103], v[212:215], v[40:43]\n\t"
      "buffer_store_dwordx4 v[136:139], %[va], %[rA], %[x2] offen offset:1024 sc1\n\t"
      "buffer_store_dwordx2 v[140:141], %[l8], %[rA], %[x2] offen offset:0 sc1\n\t"
      "s_waitcnt lgkmcnt(12)\n\t"
      "v_mfma_f32_16x16x32_bf16 v[4:7], v[104:107], v[208:211], v[4:7]\n\t"
      "ds_read_b64_tr_b16 v[96:97], %[w0] offset:128\n\t"
      "v_mfma_f32_16x16x32_bf16 v[44:47], v[104:107], v[212:215], v[44:47]\n\t"
      "ds_read_b64_tr_b16 v[98:99], %[w0] offset:5248\n\t"
      "v_mfma_f32_16x16x32_bf16 v[8:11], v[108:111], v[208:211], v[8:11]\n\t"
      "ds_read_b64_tr_b16 v[100:101], %[w0] offset:10240\n\t"
      "v_mfma_f32_16x16x32_bf16 v[48:51], v[108:111], v[212:215], v[48:51]\n\t"
      "ds_read_b64_tr_b16 v[102:103], %[w0] offset:15360\n\t"
      "s_waitcnt lgkmcnt(12)\n\t"
      "v_mfma_f32_16x16x32_bf16 v[12:15], v[112:115], v[208:211], v[12:15]\n\t"
      "ds_read_b64_tr_b16 v[104:105], %[w1] offset:10240\n\t"
      "v_mfma_f32_16x16x32_bf16 v[52:55], v[112:115], v[212:215], v[52:55]\n\t"
      "ds_read_b64_tr_b16 v[106:107], %[w1] offset:15360\n\t"
      "v_mfma_f32_16x16x32_bf16 v[16:19], v[116:119], v[208:211], v[16:19]\n\t"
      "ds_read_b64_tr_b16 v[108:109], %[w0] offset:10304\n\t"
      "v_mfma_f32_16x16x32_bf16 v[56:59], v[116:119], v[212:215], v[56:59]\n\t"
      "ds_read_b64_tr_b16 v[110:111], %[w0] offset:15424\n\t"
      "s_waitcnt lgkmcnt(12)\n\t"
      "v_mfma_f32_16x16x32_bf16 v[0:3], v[80:83], v[216:219], v[0:3]\n\t"
      "ds_read_b64_tr_b16 v[112:113], %[w1] offset:10304\n\t"
      "v_mfma_f32_16x16x32_bf16 v[40:43], v[80:83], v[220:223], v[40:43]\n\t"
      "ds_read_b64_tr_b16 v[114:115], %[w1] offset:15424\n\t"
      "v_mfma_f32_16x16x32_bf16 v[4:7], v[84:87], v[216:219], v[4:7]\n\t"
      "ds_read_b64_tr_b16 v[116:117], %[w0] offset:10368\n\t"
      "v_mfma_f32_16x16x32_bf16 v[44:47], v[84:87], v[220:223], v[44:47]\n\t"
      "ds_read_b64_tr_b16 v[118:119], %[w0] offset:15488\n\t"
      "s_waitcnt lgkmcnt(12)\n\t"
      "v_mfma_f32_16x16x32_bf16 v[8:11], v[88:91], v[216:219], v[8:11]\n\t"
      "v_mfma_f32_16x16x32_bf16 v[48:51], v[88:91], v[220:223], v[48:51]\n\t"
      "v_mfma_f32_16x16x32_bf16 v[12:15], v[92:95], v[216:219], v[12:15]\n\t"
      "v_mfma_f32_16x16x32_bf16 v[52:55], v[92:95], v[220:223], v[52:55]\n\t"
      "s_waitcnt lgkmcnt(8)\n\t"
      "v_mfma_f32_16x16x32_bf16 v[16:19], v[96:99], v[216:219], v[16:19]\n\t"
      "v_mfma_f32_16x16x32_bf16 v[56:59], v[96:99], v[220:223], v[56:59]\n\t"
      "v_mfma_f32_16x16x32_bf16 v[0:3], v[100:103], v[224:227], v[0:3]\n\t"
      "v_mfma_f32_16x16x32_bf16 v[40:43], v[100:103], v[228:231], v[40:43]\n\t"
      "s_waitcnt lgkmcnt(4)\n\t"
      "v_mfma_f32_16x16x32_bf16 v[4:7], v[104:107], v[224:227], v[4:7]\n\t"
      "v_mfma_f32_16x16x32_bf16 v[44:47], v[104:107], v[228:231], v[44:47]\n\t"
      "v_mfma_f32_16x16x32_bf16 v[8:11], v[108:111], v[224:227], v[8:11]\n\t"
      "v_mfma_f32_16x16x32_bf16 v[48:51], v[108:111], v[228:231], v[48:51]\n\t"
      "s_waitcnt lgkmcnt(0)\n\t"
      "v_mfma_f32_16x16x32_bf16 v[12:15], v[112:115], v[224:227], v[12:15]\n\t"
      "v_mfma_f32_16x16x32_bf16 v[52:55], v[112:115], v[228:231], v[52:55]\n\t"
      "v_mfma_f32_16x16x32_bf16 v[16:19], v[116:119], v[224:227], v[16:19]\n\t"
      "v_mfma_f32_16x16x32_bf16 v[56:59], v[116:119], v[228:231], v[56:59]\n\t"
      "s_setprio 3\n\t"
      "s_nop 7\n\t"
        : "=&{v[0:31]}"(r0), "=&{v[32:63]}"(r1), "=&{v[64:79]}"(r2), [so1] "+s"(so1), [so2] "+s"(so2), [w0] "+v"(w0), [w1] "+v"(w1), [w2] "+v"(w2), [w3] "+v"(w3),
          [cnt] "=&s"(cnt) AST_OUT
        : [fa] "v"(fa), [tag] "s"(tag), [inc] "s"(inc), [va] "v"(va), [pw] "v"(pw), [po] "v"(po), [rA] "s"(rWS), [l8] "v"(l8), [x1] "s"(x1), [x2] "s"(x2), [isq] "s"(isQ)
        : "memory", "scc", "vcc", "v80", "v81", "v82", "v83", "v84", "v85", "v86", "v87", "v88", "v89", "v90", "v91", "v92", "v93", "v94", "v95", "v96", "v97", "v98", "v99", "v100", "v101", "v102", "v103", "v104", "v105", "v106", "v107", "v108", "v109", "v110", "v111", "v112", "v113", "v114", "v115", "v116", "v117", "v118", "v119", "v120", "v121", "v122", "v123", "v124", "v125", "v126", "v127", "v128", "v129", "v130", "v131", "v132", "v133", "v134", "v135", "v136", "v137", "v138", "v139", "v140", "v141", "v142", "v143", "v144", "v145", "v146", "v147", "v148", "v149", "v150", "v151", "v152", "v153", "v154", "v155", "v156", "v157", "v158", "v159", "v160", "v161", "v162", "v163", "v164", "v165", "v166", "v167", "v168", "v169", "v170", "v171", "v172", "v173", "v174", "v175", "v176", "v177", "v178", "v179", "v180", "v181", "v182", "v183", "v184", "v185", "v186", "v187", "v188", "v189", "v190", "v191", "v192", "v193", "v194", "v195", "v196", "v197", "v198", "v199", "v200", "v201", "v202", "v203", "v204", "v205", "v206", "v207", "v208", "v209", "v210", "v211", "v212", "v213", "v214", "v215", "v216", "v217", "v218", "v219", "v220", "v221", "v222", "v223", "v224", "v225", "v226", "v227", "v228", "v229", "v230", "v231", "v232", "v233");
      STAMP(7); AST_PUT(20); }
    tail(x, ws, out, tag, wave, va, pw, r0, r1, r2 STAMP_PASS);
  }
}

extern "C" void kernel_launch(void* const* d_in, const int* in_sizes, int n_in, void* d_out, int out_size, void* d_ws, size_t ws_size,
                              hipStream_t stream) {
  (void)hipFuncSetAttribute((const void*)fused, hipFuncAttributeMaxDynamicSharedMemorySize, LDS_BYTES);
  fused<<<256, 512, LDS_BYTES, stream>>>((const u16*)d_in[0], (const u16*)d_in[1], (const u16*)d_in[5], (char*)d_ws + (WSTOP ? (ws_size - WS_END) & ~(size_t)0xFFFFF : 0), (const u16*)d_in[2],
                                         (const u16*)d_in[3], (const u16*)d_in[6], (const u16*)d_in[7], (const u16*)d_in[8], (u16*)d_out);
}
```
